```python
import torch

def _softmax(x, axis=-1):
    return torch.softmax(x, dim=axis)

def _gelu(x):
    return torch.nn.functional.gelu(x, approximate='tanh')

def _silu(x):
    return torch.nn.functional.silu(x)

def _sigmoid(x):
    return torch.sigmoid(x)

def _layernorm(x, axis=-1, eps=1e-05):
    m = x.mean(dim=axis, keepdim=True)
    v = x.var(dim=axis, keepdim=True, unbiased=False)
    return (x - m) / torch.sqrt(v + eps)

def _scatter_add(tgt, idx, src, axis=0):
    return tgt.clone().index_add_(axis, idx, src)

def _segment_sum(src, seg, n):
    out = torch.zeros((n,) + src.shape[1:], dtype=src.dtype)
    return out.index_add_(0, seg, src)

def _pad(x, pw):
    flat = tuple((v for pair in reversed(pw) for v in pair))
    return torch.nn.functional.pad(x, flat)
kernel_name = 'adaLN_cond__ssm_conv_h3__mixer_s_mlp__prompt'

def setup_inputs():

    _w = lambda a: (lambda t: t.to(torch.bfloat16) if t.is_floating_point() else t)(torch.as_tensor(a))

    def _A_setup():
        B, S, D = (1, 2048, 512)
        ks = 4
        return {'q': _w((lambda *s: torch.randn(*s, dtype=torch.bfloat16) * 0.05)(B, S, D)), 'k': _w((lambda *s: torch.randn(*s, dtype=torch.bfloat16) * 0.8)(B, S, D)), 'v': _w((lambda *s: torch.randn(*s, dtype=torch.bfloat16) * 0.8)(B, S, D)), 'shift_k': _w((lambda *s: torch.randn(*s, dtype=torch.bfloat16) * 0.8)(D, ks) * 0.5), 'ssm_k': _w((lambda *s: torch.randn(*s, dtype=torch.bfloat16) * 0.8)(D, S) * (1.0 / S ** 0.5)), 'ssm_D': _w((lambda *s: torch.randn(*s, dtype=torch.bfloat16) * 0.8)(D))}
    _A_in = _A_setup()

    def _B_setup():
        B, S, D, F = (1, 2048, 512, 1792)
        return {'x': _w((lambda *s: torch.randn(*s, dtype=torch.bfloat16) * 0.05)(B, S, D)), 'w1': _w((lambda *s: torch.randn(*s, dtype=torch.bfloat16) * 3.2)(D, D) * D ** (-0.5)), 'w2': _w((lambda *s: torch.randn(*s, dtype=torch.bfloat16) * 3.2)(D, D) * D ** (-0.5)), 'w3': _w((lambda *s: torch.randn(*s, dtype=torch.bfloat16) * 3.2)(D, F) * D ** (-0.5)), 'w4': _w((lambda *s: torch.randn(*s, dtype=torch.bfloat16) * 3.2)(F, D) * F ** (-0.5))}
    _B_in = _B_setup()
    return {'A_q': _A_in['q'], 'A_k': _A_in['k'], 'A_v': _A_in['v'], 'A_shift_k': _A_in['shift_k'], 'A_ssm_k': _A_in['ssm_k'], 'A_ssm_D': _A_in['ssm_D'], 'B_x': _B_in['x'], 'B_w1': _B_in['w1'], 'B_w2': _B_in['w2'], 'B_w3': _B_in['w3'], 'B_w4': _B_in['w4'], 'c': _w((lambda *s: torch.randn(*s, dtype=torch.bfloat16) * 0.1)(1, 512) * 8.0), 'w_mod': _w((lambda *s: torch.randn(*s, dtype=torch.bfloat16) * 0.1)(512, 6 * 512) * 512 ** (-0.5) * 8.0)}

def reference(A_q, A_k, A_v, A_shift_k, A_ssm_k, A_ssm_D, B_x, B_w1, B_w2, B_w3, B_w4, c, w_mod):

    def _A_ref(q, k, v, shift_k, ssm_k, ssm_D):
        B, S, D = q.shape
        ks = shift_k.shape[-1]
        k_pad = _pad(k, ((0, 0), (ks - 1, 0), (0, 0)))
        k_shift = torch.zeros_like(k)
        for t in range(ks):
            k_shift = k_shift + k_pad[:, t:t + S, :] * shift_k[:, ks - 1 - t][None, None, :]
        kv = k_shift * v
        L = 2 * S
        kv_pad = _pad(kv, ((0, 0), (0, S), (0, 0)))
        kf_pad = _pad(ssm_k, ((0, 0), (0, S)))
        kv_f = (lambda x, axes=(-2, -1): torch.fft.rfft2(x.float(), dim=axes))(kv_pad[:, None, :, :], axes=(1, 2))
        kf_f = (lambda x, axes=(-2, -1): torch.fft.rfft2(x.float(), dim=axes))(kf_pad[:, None, :], axes=(1, 2))
        kf_f = (lambda x, *d: x.permute(*d))(kf_f, 1, 2, 0)
        y_f = kv_f * kf_f[None, :, :, :]
        y = (lambda x, s, axes=(-2, -1): torch.fft.irfft2(x, s=s, dim=axes).to(torch.bfloat16))(y_f, (1, L), axes=(1, 2))[:, 0, :S, :]
        y = y + kv * ssm_D[None, None, :]
        return q * y

    def _B_ref(x, w1, w2, w3, w4):
        B, S, D = x.shape
        h = _gelu(_layernorm(x, axis=-1) @ w1)
        c = D // 4
        g0 = _pad(h[:, :, 0 * c:1 * c], ((0, 0), (1, 0), (0, 0)))[:, :S, :]
        g1 = _pad(h[:, :, 1 * c:2 * c], ((0, 0), (0, 1), (0, 0)))[:, 1:, :]
        g2 = _pad(h[:, :, 2 * c:3 * c], ((0, 0), (2, 0), (0, 0)))[:, :S, :]
        g3 = _pad(h[:, :, 3 * c:], ((0, 0), (0, 2), (0, 0)))[:, 2:, :]
        hs = torch.concatenate([g0, g1, g2, g3], axis=-1) @ w2
        x1 = x + hs
        h2 = _gelu(_layernorm(x1, axis=-1) @ w3) @ w4
        return x1 + h2
    x = A_q
    mod = (_silu(c) @ w_mod).reshape(c.shape[0], 6, 1, -1)
    sa, ca, ga, sb, cb, gb = (mod[:, 0].clone(), mod[:, 1].clone(), mod[:, 2].clone(), mod[:, 3].clone(), mod[:, 4].clone(), mod[:, 5].clone())
    h = _layernorm(x, axis=-1) * (1 + ca) + sa
    x = x + ga * _A_ref(h, A_k, A_v, A_shift_k, A_ssm_k, A_ssm_D)
    h = _layernorm(x, axis=-1) * (1 + cb) + sb
    x = x + gb * _B_ref(h, B_w1, B_w2, B_w3, B_w4)
    return x
```

```cpp
#include <hip/hip_runtime.h>
#include <hip/hip_bf16.h>
#include <stdint.h>

typedef unsigned short u16;
#define S_ 2048
#define D_ 512
#define F_ 1792

__device__ __forceinline__ float bf2f(u16 b){ return __builtin_bit_cast(float, ((unsigned)b)<<16); }
__device__ __forceinline__ u16 f2bf(float f){ unsigned u=__builtin_bit_cast(unsigned,f); u += 0x7FFFu + ((u>>16)&1u); return (u16)(u>>16); }
__device__ __forceinline__ float rb(float f){ return bf2f(f2bf(f)); }

__global__ void k_mod(const u16* c, const u16* wmod, float* mod){
  int n = blockIdx.x*blockDim.x+threadIdx.x;
  if(n>=3072) return;
  float acc=0.f;
  for(int k=0;k<512;k++){
    float x=bf2f(c[k]); float s=rb(x/(1.f+__expf(-x)));
    acc += s*bf2f(wmod[k*3072+n]);
  }
  mod[n]=rb(acc);
}
__global__ void k_kv(const u16* k, const u16* v, const u16* sk, u16* kv){
  int i = blockIdx.x*blockDim.x+threadIdx.x;
  int s=i/D_, d=i%D_;
  float acc=0.f;
  for(int t=0;t<4;t++){
    int ss=s+t-3;
    float kk = ss>=0? bf2f(k[ss*D_+d]):0.f;
    acc = rb(acc + rb(kk*bf2f(sk[d*4+3-t])));
  }
  kv[i]=f2bf(acc*bf2f(v[i]));
}
__global__ void k_conv(const u16* kv, const u16* ssmk, const u16* ssmD, u16* z){
  int i = blockIdx.x*blockDim.x+threadIdx.x;
  int s=i/D_, d=i%D_;
  float acc=0.f;
  for(int j=0;j<=s;j++) acc += bf2f(ssmk[d*S_+j])*bf2f(kv[(s-j)*D_+d]);
  float y=rb(acc);
  z[i]=f2bf(y+rb(bf2f(kv[i])*bf2f(ssmD[d])));
}
__device__ float blk_sum(float v, float* sm){
  for(int o=32;o>0;o>>=1) v+=__shfl_xor(v,o);
  __syncthreads();
  if((threadIdx.x&63)==0) sm[threadIdx.x>>6]=v;
  __syncthreads();
  float t=0; for(int i=0;i<8;i++) t+=sm[i];
  return t;
}
__device__ float ln(float x, float* sm){
  float m=blk_sum(x,sm)*(1.f/512);
  float dv=x-m;
  float var=blk_sum(dv*dv,sm)*(1.f/512);
  float mb=rb(m), vb=rb(var);
  float sd=rb(sqrtf(rb(vb+1e-5f)));
  return rb(rb(x-mb)/sd);
}
__global__ void k_row1(const u16* q, const u16* z, const float* mod, u16* x1, u16* h2, u16* a1){
  __shared__ float sm[8];
  int s=blockIdx.x, d=threadIdx.x;
  float x=bf2f(q[s*D_+d]);
  float sa=mod[d], ca=mod[512+d], ga=mod[1024+d], sb=mod[1536+d], cb=mod[2048+d];
  float h=rb(rb(ln(x,sm)*rb(1.f+ca))+sa);
  float a=rb(h*bf2f(z[s*D_+d]));
  float xx=rb(x+rb(ga*a));
  x1[s*D_+d]=f2bf(xx);
  float hh=rb(rb(ln(xx,sm)*rb(1.f+cb))+sb);
  h2[s*D_+d]=f2bf(hh);
  a1[s*D_+d]=f2bf(ln(hh,sm));
}
__device__ float gelu(float x){
  float u=0.7978845608028654f*(x+0.044715f*x*x*x);
  return 0.5f*x*(1.f+tanhf(u));
}
template<int MODE>
__global__ void k_gemm(const u16* A, const u16* W, u16* C, int N, int K, const u16* r1, const u16* r2, const float* mod){
  int n=blockIdx.x*blockDim.x+threadIdx.x, s=blockIdx.y;
  float acc=0.f;
  for(int k=0;k<K;k++){
    int ss=s;
    if(MODE==1){ int g=k>>7; ss = g==0? s-1: g==1? s+1: g==2? s-2: s+2; }
    float a = (ss>=0 && ss<S_)? bf2f(A[ss*K+k]):0.f;
    acc += a*bf2f(W[k*N+n]);
  }
  acc=rb(acc);
  if(MODE==0) C[s*N+n]=f2bf(gelu(acc));
  if(MODE==1) C[s*N+n]=f2bf(bf2f(r1[s*N+n])+acc);
  if(MODE==2){ float b=rb(bf2f(r1[s*N+n])+acc); C[s*N+n]=f2bf(bf2f(r2[s*N+n])+rb(mod[2560+n]*b)); }
}
__global__ void k_ln(const u16* x, u16* y){
  __shared__ float sm[8];
  int s=blockIdx.x, d=threadIdx.x;
  y[s*D_+d]=f2bf(ln(bf2f(x[s*D_+d]),sm));
}

extern "C" void kernel_launch(void* const* d_in, const int* in_sizes, int n_in,
                              void* d_out, int out_size, void* d_ws, size_t ws_size,
                              hipStream_t stream) {
  const u16* q=(const u16*)d_in[0]; const u16* k=(const u16*)d_in[1]; const u16* v=(const u16*)d_in[2];
  const u16* sk=(const u16*)d_in[3]; const u16* ssmk=(const u16*)d_in[4]; const u16* ssmD=(const u16*)d_in[5];
  const u16* w1=(const u16*)d_in[7]; const u16* w2=(const u16*)d_in[8]; const u16* w3=(const u16*)d_in[9]; const u16* w4=(const u16*)d_in[10];
  const u16* c=(const u16*)d_in[11]; const u16* wmod=(const u16*)d_in[12];
  char* ws=(char*)d_ws;
  float* mod=(float*)ws;
  size_t MB2=S_*D_*2;
  u16* kv=(u16*)(ws+65536); u16* z=(u16*)(ws+65536+MB2); u16* x1=(u16*)(ws+65536+2*MB2); u16* h2=(u16*)(ws+65536+3*MB2);
  u16* a1=(u16*)(ws+65536+4*MB2); u16* hh=(u16*)(ws+65536+5*MB2); u16* x1p=(u16*)(ws+65536+6*MB2); u16* a3=(u16*)(ws+65536+7*MB2);
  u16* g=(u16*)(ws+65536+8*MB2);
  k_mod<<<12,256,0,stream>>>(c,wmod,mod);
  k_kv<<<S_*D_/256,256,0,stream>>>(k,v,sk,kv);
  k_conv<<<S_*D_/256,256,0,stream>>>(kv,ssmk,ssmD,z);
  k_row1<<<S_,512,0,stream>>>(q,z,mod,x1,h2,a1);
  k_gemm<0><<<dim3(2,S_),256,0,stream>>>(a1,w1,hh,512,512,nullptr,nullptr,mod);
  k_gemm<1><<<dim3(2,S_),256,0,stream>>>(hh,w2,x1p,512,512,h2,nullptr,mod);
  k_ln<<<S_,512,0,stream>>>(x1p,a3);
  k_gemm<0><<<dim3(7,S_),256,0,stream>>>(a3,w3,g,F_,512,nullptr,nullptr,mod);
  k_gemm<2><<<dim3(2,S_),256,0,stream>>>(g,w4,(u16*)d_out,512,F_,x1p,x1,mod);
}
```

```cpp
#include <hip/hip_runtime.h>
#include <stdint.h>

namespace mk {
typedef unsigned short u16;
typedef unsigned v4u __attribute__((ext_vector_type(4)));
typedef unsigned v2u __attribute__((ext_vector_type(2)));
typedef float f32x4 __attribute__((ext_vector_type(4)));
typedef float f32x2 __attribute__((ext_vector_type(2)));
typedef __bf16 bf16x8 __attribute__((ext_vector_type(8)));
typedef __bf16 bf16x2 __attribute__((ext_vector_type(2)));
typedef short s16x4 __attribute__((ext_vector_type(4)));
typedef __amdgpu_buffer_rsrc_t rsrc_t;
#define LAS __attribute__((address_space(3)))
#define GAS __attribute__((address_space(1)))
#define DEVI __device__ __forceinline__

constexpr int SC1 = 16;
constexpr int OFF_EPOCH = 0;
constexpr int OFF_F0 = 4096;
constexpr int OFF_FR = OFF_F0 + 1024 * 128;
constexpr int OFF_F1 = OFF_FR + 8 * 8192;
constexpr int OFF_F2 = OFF_F1 + 8 * 8192;
constexpr int OFF_F3 = OFF_F2 + 256 * 128;
constexpr int OFF_F4 = OFF_F3 + 256 * 128;
constexpr int OFF_MOD = OFF_F4 + 256 * 128;
constexpr int OFF_UR = 1 << 20;
constexpr int OFF_Z = OFF_UR + (2 << 20);
constexpr int OFF_W1P = OFF_Z + (2 << 20);
constexpr int OFF_W2P = OFF_W1P + (512 << 10);
constexpr int OFF_W4P = OFF_W2P + (512 << 10);
constexpr int OFF_HH = OFF_W4P + (2 << 20);
constexpr int OFF_X1P = OFF_HH + (2 << 20);
constexpr int OFF_R = OFF_X1P + (2 << 20);
constexpr int OFF_G = OFF_R + (4 << 20);
constexpr int WS_USED = OFF_G + 128 * 57344;
constexpr unsigned OOB = 0x80000000u;
constexpr int L_W3 = 0;
constexpr int L_KC = 112256;
constexpr int L_UC = L_KC + 8 * 4160;
constexpr int L_X = 114688;
constexpr int L_X1 = L_X + 32768;
constexpr int L_PART = L_X1 + 8192;
constexpr int L_CS3 = L_PART + 3 * 2048;
constexpr int L_CNT = 106496;
static_assert(L_UC + 4 * 4576 == 163840, "lds");
static_assert(L_CS3 + 448 <= 163840, "lds");

#define STAMP(i) do {} while (0)

struct Params {
  const u16 *q, *k, *v, *sk, *ssmk, *ssmD, *w1, *w2, *w3, *w4, *c, *wmod;
  u16* out;
  char* ws;
};

DEVI float lo(unsigned u) { return __builtin_bit_cast(float, u << 16); }
DEVI float hi(unsigned u) { return __builtin_bit_cast(float, u & 0xffff0000u); }
DEVI unsigned pk(float a, float b) { f32x2 f = {a, b}; return __builtin_bit_cast(unsigned, __builtin_convertvector(f, bf16x2)); }
DEVI float rb(float a) { return lo(pk(a, a)); }
DEVI f32x4 mfma(v4u a, v4u b, f32x4 c) {
  return __builtin_amdgcn_mfma_f32_16x16x32_bf16(__builtin_bit_cast(bf16x8, a), __builtin_bit_cast(bf16x8, b), c, 0, 0, 0);
}
DEVI rsrc_t mkrs(const void* p, int bytes) { return __builtin_amdgcn_make_buffer_rsrc((void*)p, 0, bytes, 0x00020000); }
DEVI v4u ld128(rsrc_t r, unsigned off, int aux) { return aux ? __builtin_amdgcn_raw_buffer_load_b128(r, off, 0, SC1) : __builtin_amdgcn_raw_buffer_load_b128(r, off, 0, 0); }
DEVI v2u ld64s(rsrc_t r, unsigned off) { return __builtin_amdgcn_raw_buffer_load_b64(r, off, 0, SC1); }
DEVI unsigned ld32(rsrc_t r, unsigned off, int aux) { return aux ? __builtin_amdgcn_raw_buffer_load_b32(r, off, 0, SC1) : __builtin_amdgcn_raw_buffer_load_b32(r, off, 0, 0); }
DEVI void st128s(v4u v, rsrc_t r, unsigned off) { __builtin_amdgcn_raw_buffer_store_b128(v, r, off, 0, SC1); }
DEVI void st32s(unsigned v, rsrc_t r, unsigned off) { __builtin_amdgcn_raw_buffer_store_b32(v, r, off, 0, SC1); }
#define MEMFENCE() asm volatile("" ::: "memory")
#define DRAIN() asm volatile("s_waitcnt vmcnt(0)" ::: "memory")
#define BAR() asm volatile("s_waitcnt lgkmcnt(0)\n\ts_barrier" ::: "memory")
#define SCHED() __builtin_amdgcn_sched_barrier(0)
constexpr unsigned kSpinCap = 1u << 21;

DEVI void poll1(rsrc_t r, unsigned off, unsigned tag) {
  for (unsigned s = 0; s < kSpinCap; ++s) {
    MEMFENCE();
    const unsigned f = ld32(r, off, 1);
    if (__all(f == tag)) break;
  }
  MEMFENCE();
}
DEVI void poll4(rsrc_t r, unsigned off, unsigned tag) {
  for (unsigned s = 0; s < kSpinCap; ++s) {
    MEMFENCE();
    const v4u f = ld128(r, off, 1);
    if (__all((f.x == tag) & (f.y == tag) & (f.z == tag) & (f.w == tag))) break;
  }
  MEMFENCE();
}
DEVI float fbits(unsigned u) { return __builtin_bit_cast(float, u); }
DEVI unsigned ubits(float f) { return __builtin_bit_cast(unsigned, f); }
DEVI float ror8(float x) { return fbits(__builtin_amdgcn_update_dpp(0u, ubits(x), 0x128, 0xf, 0xf, false)); }
DEVI void swap32(float& a, float& b) { auto r = __builtin_amdgcn_permlane32_swap(ubits(a), ubits(b), false, false); a = fbits(r[0]); b = fbits(r[1]); }
DEVI void swap16(float& a, float& b) { auto r = __builtin_amdgcn_permlane16_swap(ubits(a), ubits(b), false, false); a = fbits(r[0]); b = fbits(r[1]); }
DEVI void swap16u(unsigned& a, unsigned& b) { auto r = __builtin_amdgcn_permlane16_swap(a, b, false, false); a = r[0]; b = r[1]; }

DEVI float ln_wave_reduce(const float (&s)[4], const float (&qv)[4], int lane) {
  float a0 = s[0], b0 = s[2], a1 = s[1], b1 = s[3], c0 = qv[0], d0 = qv[2], c1 = qv[1], d1 = qv[3];
  swap32(a0, b0); swap32(a1, b1); swap32(c0, d0); swap32(c1, d1);
  float s0 = a0 + b0, s1 = a1 + b1, q0 = c0 + d0, q1 = c1 + d1;
  swap16(s0, s1); swap16(q0, q1);
  const float x = s0 + s1, y = q0 + q1;
  const bool b3 = lane & 8;
  return (b3 ? y : x) + ror8(b3 ? x : y);
}
DEVI void ln_finish(float sum, float sq, float& mean, float& rstd) {
  const float m = sum * (1.f / 512.f);
  const float var = fmaxf(sq * (1.f / 512.f) - m * m, 0.f);
  mean = rb(m);
  rstd = __builtin_amdgcn_rcpf(rb(__builtin_amdgcn_sqrtf(rb(rb(var) + 1e-5f))));
}
DEVI void ln_total(LAS const float* part, int lane, float& mean, float& rstd) {
  float t = 0.f;
#pragma unroll
  for (int w = 0; w < 8; ++w) t += part[w * 64 + lane];
  const float o = ror8(t);
  const bool b3 = lane & 8;
  ln_finish(b3 ? o : t, b3 ? t : o, mean, rstd);
}
DEVI void gather4(float x, float (&o)[4]) {
  float e = x, d = x;
  swap16(e, d);
  float e2 = e, d2 = d;
  swap32(e, e2); swap32(d, d2);
  o[0] = e; o[2] = e2; o[1] = d; o[3] = d2;
}
DEVI float gelu(float x) {
  const float t = x * (2.302208198f + 0.1029432397f * x * x);
  return x * __builtin_amdgcn_rcpf(1.f + __builtin_amdgcn_exp2f(-t));
}
DEVI void glds16(const void* gsrc, unsigned lds_dst) {
  unsigned keep;
  asm volatile("s_mov_b32 %0, m0\n\ts_mov_b32 m0, %2\n\ts_nop 0\n\tglobal_load_lds_dwordx4 %1, off\n\ts_mov_b32 m0, %0" : "=&s"(keep) : "v"(gsrc), "s"(lds_dst) : "memory");
}
DEVI v2u trread(LAS char* a) { return __builtin_bit_cast(v2u, __builtin_amdgcn_ds_read_tr16_b64_v4i16((LAS s16x4*)a)); }

__global__ void __launch_bounds__(512, 2) mega(Params p) {
  __shared__ __attribute__((aligned(16))) char smem[163840];
  LAS char* L = (LAS char*)smem;
  const int tid = threadIdx.x, lane = tid & 63, w = __builtin_amdgcn_readfirstlane(tid >> 6);
  const int b = blockIdx.x, team = b & 7, rank = b >> 3, me = team * 32 + rank;
  const int c16 = lane & 15, qq = lane >> 4;
  const rsrc_t ws = mkrs(p.ws, WS_USED);
  STAMP(0);
  if (tid == 0) *(LAS unsigned*)(L + L_CNT) = 0u;
  const unsigned epoch = ld32(ws, OFF_EPOCH, 1);
  BAR();

  const rsrc_t rssm = mkrs(p.ssmk, 512 * 2048 * 2);
  v4u kp0, kp1;
  if (w < 4) {
    const rsrc_t rk = mkrs(p.k, 2048 * 1024), rv = mkrs(p.v, 2048 * 1024);
    const int tg = lane & 7, d0 = 64 * team + 2 * (8 * w + (lane >> 3)), s0 = 64 * rank + 8 * tg;
    unsigned kk[11], vv[8];
#pragma unroll
    for (int i = 0; i < 3; ++i) kk[i] = ld32(rk, s0 ? (unsigned)((s0 - 3) * 1024 + d0 * 2) + i * 1024 : OOB, 0);
#pragma unroll
    for (int i = 3; i < 11; ++i) kk[i] = ld32(rk, (unsigned)(s0 * 1024 + d0 * 2) + (i - 3) * 1024, 0);
#pragma unroll
    for (int i = 0; i < 8; ++i) vv[i] = ld32(rv, (unsigned)((s0 + i) * 1024 + d0 * 2), 0);
    const v4u sk = *(const GAS v4u*)(p.sk + d0 * 4);
    {
      const int ch = tid >> 8, a = tid & 255;
      const unsigned o = (unsigned)((2 * me + ch) * 4096 + a * 16);
      kp1 = ld128(rssm, o, 0);
      kp0 = ld128(rssm, a ? o - 16 : OOB, 0);
    }
    const float a0 = lo(sk.x), a1 = hi(sk.x), a2 = lo(sk.y), a3 = hi(sk.y), b0 = lo(sk.z), b1 = hi(sk.z), b2 = lo(sk.w), b3 = hi(sk.w);
    float x0[8], x1[8];
#pragma unroll
    for (int t = 0; t < 8; ++t) {
      x0[t] = (lo(kk[t]) * a3 + lo(kk[t + 1]) * a2 + lo(kk[t + 2]) * a1 + lo(kk[t + 3]) * a0) * lo(vv[t]);
      x1[t] = (hi(kk[t]) * b3 + hi(kk[t + 1]) * b2 + hi(kk[t + 2]) * b1 + hi(kk[t + 3]) * b0) * hi(vv[t]);
    }
    const v4u o0 = {pk(x0[7], x0[6]), pk(x0[5], x0[4]), pk(x0[3], x0[2]), pk(x0[1], x0[0])};
    const v4u o1 = {pk(x1[7], x1[6]), pk(x1[5], x1[4]), pk(x1[3], x1[2]), pk(x1[1], x1[0])};
    const unsigned uo = OFF_UR + d0 * 4096 + (2040 - s0) * 2;
    st128s(o0, ws, uo);
    st128s(o1, ws, uo + 4096);
    DRAIN();
    const unsigned tag0 = epoch * 0x9E3779B1u + 0x7F4A7C15u;
    if (lane == 0) st32s(tag0, ws, OFF_F0 + ((team * 4 + w) * 32 + rank) * 128);
    STAMP(1);
  } else {
    {
      const int ch = tid >> 8, a = tid & 255;
      const unsigned o = (unsigned)((2 * me + ch) * 4096 + a * 16);
      kp1 = ld128(rssm, o, 0);
      kp0 = ld128(rssm, a ? o - 16 : OOB, 0);
    }
    if (w == 4) {
      if (rank < 24) {
        const int n0 = 384 * team + 16 * rank + 8 * (lane & 1), rg = lane >> 1;
        v4u wv[16];
#pragma unroll
        for (int i = 0; i < 16; ++i) wv[i] = *(const GAS v4u*)(p.wmod + (16 * rg + i) * 3072 + n0);
        const v4u c0 = *(const GAS v4u*)(p.c + 16 * rg), c1 = *(const GAS v4u*)(p.c + 16 * rg + 8);
        float acc[8] = {0.f, 0.f, 0.f, 0.f, 0.f, 0.f, 0.f, 0.f};
#pragma unroll
        for (int i = 0; i < 16; ++i) {
          const unsigned cu = i < 8 ? c0[i >> 1] : c1[(i - 8) >> 1];
          const float x = (i & 1) ? hi(cu) : lo(cu);
          const float s = rb(x * __builtin_amdgcn_rcpf(1.f + __builtin_amdgcn_exp2f(-1.442695041f * x)));
#pragma unroll
          for (int j = 0; j < 4; ++j) { acc[2 * j] += s * lo(wv[i][j]); acc[2 * j + 1] += s * hi(wv[i][j]); }
        }
#pragma unroll
        for (int o = 2; o < 64; o <<= 1)
#pragma unroll
          for (int j = 0; j < 8; ++j) acc[j] += __shfl_xor(acc[j], o);
        if (lane < 2) st128s(v4u{pk(acc[0], acc[1]), pk(acc[2], acc[3]), pk(acc[4], acc[5]), pk(acc[6], acc[7])}, ws, OFF_MOD + n0 * 2);
      }
    } else if (w < 7) {
      const int u = (w - 5) * 256 + b;
      if (u < 352) {
        const u16* src; int cb, ks; unsigned dst; int k0, k1;
        if (u < 128) {
          src = u < 64 ? p.w1 : p.w2; cb = (u >> 4) & 3; ks = u & 15;
          dst = (u < 64 ? OFF_W1P : OFF_W2P) + (cb * 8 * 16 + ks) * 1024;
          k0 = 32 * ks + 8 * qq; k1 = k0 + 4;
        } else {
          const int v = u - 128; src = p.w4; cb = v / 56; ks = v % 56;
          dst = OFF_W4P + (cb * 8 * 56 + ks) * 1024;
          const int P = 4 * ks + qq, sl = P / 14, pp = P % 14;
          if (pp < 12) { k0 = 112 * sl + 32 * (pp >> 2) + 4 * (pp & 3); k1 = k0 + 16; }
          else { k0 = 112 * sl + 96 + 8 * (pp - 12); k1 = k0 + 4; }
        }
        const int nstride = u < 128 ? 16 * 1024 : 56 * 1024;
        v4u raw[8];
#pragma unroll
        for (int j = 0; j < 8; ++j) raw[j] = *(const GAS v4u*)(src + ((j < 4 ? k0 + j : k1 + j - 4) * 512 + 128 * cb + 8 * c16));
        LAS char* stg = L + (w - 5) * 9216;
#pragma unroll
        for (int j = 0; j < 8; ++j) *(LAS v4u*)(stg + (qq * 8 + j) * 288 + c16 * 16) = raw[j];
        asm volatile("s_waitcnt lgkmcnt(0)" ::: "memory");
#pragma unroll
        for (int i = 0; i < 8; ++i) {
          LAS char* a = stg + (qq * 8 + (c16 >> 2)) * 288 + i * 32 + (lane & 3) * 8;
          const v2u t0 = trread(a), t1 = trread(a + 4 * 288);
          st128s(v4u{t0.x, t0.y, t1.x, t1.y}, ws, dst + i * nstride + lane * 16);
        }
      }
    } else {
      unsigned x = ld32(ws, OFF_F0 + ((team * 4 + (rank >> 3)) * 32 + (lane & 31)) * 128, 1);
      x ^= ld32(ws, OFF_FR + team * 8192 + lane * 128, 1);
      x ^= ld32(ws, OFF_F1 + team * 8192 + lane * 128, 1);
      x ^= ld32(ws, OFF_F2 + (lane >> 4) * 32768 + ((me & ~15) + c16) * 128, 1);
      asm volatile("" ::"v"(x));
    }
    if (w < 7) {
      DRAIN();
      unsigned old = 0;
      if (lane == 0) old = __hip_atomic_fetch_add((LAS unsigned*)(L + L_CNT), 1u, __ATOMIC_RELAXED, __HIP_MEMORY_SCOPE_WORKGROUP);
      old = __builtin_amdgcn_readfirstlane(old);
      const unsigned tag0 = epoch * 0x9E3779B1u + 0x7F4A7C15u;
      if (old == 2 && lane < 8) st32s(tag0, ws, OFF_FR + lane * 8192 + (b >> 2) * 128 + (b & 3) * 4);
    }
  }
  const unsigned tag = __builtin_amdgcn_readfirstlane(epoch * 0x9E3779B1u + 0x7F4A7C15u);
  {
    const int ch = tid >> 8, a = tid & 255;
    LAS char* kc = L + L_KC + ch * 4 * 4160 + (a + 3) * 16;
    *(LAS v4u*)(kc) = v4u{kp0.y, kp0.z, kp0.w, kp1.x};
    *(LAS v4u*)(kc + 4160) = v4u{kp0.z, kp0.w, kp1.x, kp1.y};
    *(LAS v4u*)(kc + 2 * 4160) = v4u{kp0.w, kp1.x, kp1.y, kp1.z};
    *(LAS v4u*)(kc + 3 * 4160) = kp1;
    const v4u z4 = {0u, 0u, 0u, 0u};
    if (tid < 24) *(LAS v4u*)(L + L_KC + (tid / 3) * 4160 + (tid % 3) * 16) = z4;
    if (tid >= 128 && tid < 256 && (tid & 31) < 30) *(LAS v4u*)(L + L_UC + ((tid >> 5) & 3) * 4576 + (256 + (tid & 31)) * 16) = z4;
  }
  if (w == 0) poll1(ws, OFF_F0 + ((team * 4 + (rank >> 3)) * 32 + (lane & 31)) * 128, tag);
  BAR();
  STAMP(2);

  {
    const int ch = tid >> 8, a = tid & 255;
    const unsigned o = OFF_UR + (2 * me + ch) * 4096 + a * 16;
    const v4u u1 = ld128(ws, o, 1);
    const unsigned nx = ld32(ws, a < 255 ? o + 16 : OOB, 1);
    LAS char* uc = L + L_UC + ch * 2 * 4576 + a * 16;
    *(LAS v4u*)(uc + 4576) = u1;
    *(LAS v4u*)(uc) = v4u{__builtin_amdgcn_alignbit(u1.y, u1.x, 16), __builtin_amdgcn_alignbit(u1.z, u1.y, 16),
                          __builtin_amdgcn_alignbit(u1.w, u1.z, 16), __builtin_amdgcn_alignbit(nx, u1.w, 16)};
  }
  BAR();
  STAMP(3);
  f32x4 cacc[8];
  if (w < 4) {
    const int ch = w >> 1, t0 = 4 * (w & 1);
    LAS const char* pa = L + L_KC + (ch * 4 + (c16 & 3)) * 4160 + (qq + (c16 >> 2)) * 16 + t0 * 64;
    LAS const char* pb = L + L_UC + (ch * 2 + (c16 & 1)) * 4576 + (7 - (c16 >> 1)) * 64 + qq * 16 + t0 * 64;
#pragma unroll
    for (int g = 0; g < 8; ++g) cacc[g] = f32x4{0.f, 0.f, 0.f, 0.f};
    v4u fa[2][8], fb[2][8];
#pragma unroll
    for (int i = 0; i < 8; ++i) { fa[0][i] = *(LAS const v4u*)(pa + 512 * i); fb[0][i] = *(LAS const v4u*)(pb + 512 * (7 - i)); }
#pragma unroll
    for (int t = 0; t < 4; ++t) {
      if (t < 3) {
#pragma unroll
        for (int i = 0; i < 8; ++i) {
          fa[(t + 1) & 1][i] = *(LAS const v4u*)(pa + 64 * (t + 1) + 512 * i);
          fb[(t + 1) & 1][i] = *(LAS const v4u*)(pb + 64 * (t + 1) + 512 * (7 - i));
        }
      }
#pragma unroll
      for (int i = 0; i < 8; ++i)
#pragma unroll
        for (int n = 0; n + i < 8; ++n) cacc[i + n] = mfma(fa[t & 1][i], fb[t & 1][n], cacc[i + n]);
      if (t < 3) {
#pragma unroll
        for (int i = 0; i < 16; ++i) {
          __builtin_amdgcn_sched_group_barrier(0x100, 1, 0);
          __builtin_amdgcn_sched_group_barrier(0x8, 2, 0);
        }
        __builtin_amdgcn_sched_group_barrier(0x8, 4, 0);
      }
      SCHED();
    }
  }
  BAR();
  if (w < 4) {
#pragma unroll
    for (int g = 0; g < 8; ++g) *(LAS f32x4*)(L + L_KC + (w * 8 + g) * 1024 + lane * 16) = cacc[g];
  }
  BAR();
  STAMP(4);
  {
    const f32x4 y0 = *(LAS const f32x4*)(L + L_KC + (0 * 8 + w) * 1024 + lane * 16) + *(LAS const f32x4*)(L + L_KC + (1 * 8 + w) * 1024 + lane * 16);
    const f32x4 y1 = *(LAS const f32x4*)(L + L_KC + (2 * 8 + w) * 1024 + lane * 16) + *(LAS const f32x4*)(L + L_KC + (3 * 8 + w) * 1024 + lane * 16);
    const int a8 = 256 * w + 32 * (c16 >> 1) + 8 * qq;
    const v4u k0 = *(LAS const v4u*)(L + L_UC + 4576 + (2040 - a8) * 2), k1 = *(LAS const v4u*)(L + L_UC + 3 * 4576 + (2040 - a8) * 2);
    const unsigned dd = *(const GAS unsigned*)(p.ssmD + 2 * me);
    const float D0 = lo(dd), D1 = hi(dd);
    const int sh = (c16 & 1) ? 16 : 0;
    v4u o;
#pragma unroll
    for (int t = 0; t < 4; ++t) o[t] = pk(y0[t] + hi(k0[3 - t] << sh) * D0, y1[t] + hi(k1[3 - t] << sh) * D1);
    st128s(o, ws, OFF_Z + (8 * w + (c16 >> 1)) * 32768 + me * 128 + (2 * qq + (c16 & 1)) * 16);
  }
  DRAIN();
  BAR();
  if (tid < 8) st32s(tag, ws, OFF_F1 + tid * 8192 + (b >> 2) * 128 + (b & 3) * 4);
  STAMP(5);

  const int T = me >> 2, g = me & 3, m = me & 15, squad = me >> 4;
  const int g8 = lane >> 3, tg = lane & 7;
  if (w == 7) poll4(ws, OFF_FR + team * 8192 + lane * 128, tag);
  v4u qv[4];
#pragma unroll
  for (int t = 0; t < 4; ++t) qv[t] = *(const GAS v4u*)(p.q + (32 * T + 8 * (tg >> 1) + (tg & 1) + 2 * t) * 512 + 64 * w + 8 * g8);
  BAR();
  v4u wA[16];
  const unsigned mo = OFF_MOD + (64 * w + 8 * g8) * 2;
  const v4u msa = ld128(ws, mo, 1), mca = ld128(ws, mo + 1024, 1), mga = ld128(ws, mo + 2048, 1), msb = ld128(ws, mo + 3072, 1), mcb = ld128(ws, mo + 4096, 1);
#pragma unroll
  for (int i = 0; i < 16; ++i) wA[i] = ld128(ws, OFF_W1P + ((8 * g + w) * 16 + i) * 1024 + lane * 16, 1);
  SCHED();
  float gh[4][8];
  {
    float s[4], sq[4];
#pragma unroll
    for (int t = 0; t < 4; ++t) {
      s[t] = 0.f; sq[t] = 0.f;
#pragma unroll
      for (int i = 0; i < 4; ++i) { const float a = lo(qv[t][i]), c = hi(qv[t][i]); s[t] += a + c; sq[t] += a * a + c * c; }
    }
    ((LAS float*)(L + L_PART))[w * 64 + lane] = ln_wave_reduce(s, sq, lane);
    BAR();
    float mean, rstd, mm[4], rr[4];
    ln_total((LAS const float*)(L + L_PART), lane, mean, rstd);
    gather4(mean, mm); gather4(rstd, rr);
#pragma unroll
    for (int i = 0; i < 4; ++i) {
      const float c0 = rb(1.f + lo(mca[i])), c1 = rb(1.f + hi(mca[i]));
#pragma unroll
      for (int t = 0; t < 4; ++t) {
        gh[t][2 * i] = lo(mga[i]) * ((lo(qv[t][i]) - mm[t]) * rr[t] * c0 + lo(msa[i]));
        gh[t][2 * i + 1] = hi(mga[i]) * ((hi(qv[t][i]) - mm[t]) * rr[t] * c1 + hi(msa[i]));
      }
    }
  }
  const v4u ones = {0x3F803F80u, 0x3F803F80u, 0x3F803F80u, 0x3F803F80u};
  f32x4 cs1 = {0.f, 0.f, 0.f, 0.f};
#pragma unroll
  for (int i = 0; i < 16; ++i) cs1 = mfma(wA[i], ones, cs1);
  STAMP(6);
  if (w == 0) poll4(ws, OFF_F1 + team * 8192 + lane * 128, tag);
  BAR();
  STAMP(7);
  v4u wB[16];
  {
    v4u zz[4];
#pragma unroll
    for (int i = 0; i < 4; ++i) zz[i] = ld128(ws, OFF_Z + T * 32768 + (32 * w + 4 * g8 + i) * 128 + tg * 16, 1);
#pragma unroll
    for (int i = 0; i < 16; ++i) wB[i] = ld128(ws, OFF_W2P + ((8 * g + w) * 16 + i) * 1024 + lane * 16, 1);
    SCHED();
    float x[4][8], s[4], sq[4];
#pragma unroll
    for (int t = 0; t < 4; ++t) {
      s[t] = 0.f; sq[t] = 0.f;
#pragma unroll
      for (int i = 0; i < 4; ++i) {
        const float a = lo(qv[t][i]) + gh[t][2 * i] * lo(zz[i][t]), c = hi(qv[t][i]) + gh[t][2 * i + 1] * hi(zz[i][t]);
        x[t][2 * i] = a; x[t][2 * i + 1] = c;
        s[t] += a + c; sq[t] += a * a + c * c;
      }
    }
    ((LAS float*)(L + L_PART + 2048))[w * 64 + lane] = ln_wave_reduce(s, sq, lane);
    if ((w >> 1) == g) {
#pragma unroll
      for (int t = 0; t < 4; ++t)
        *(LAS v4u*)(L + L_X1 + (t >> 1) * 4096 + ((w & 1) * 8 + g8) * 256 + ((t & 1) * 8 + tg) * 16) =
            v4u{pk(x[t][0], x[t][1]), pk(x[t][2], x[t][3]), pk(x[t][4], x[t][5]), pk(x[t][6], x[t][7])};
    }
    BAR();
    float mean, rstd, mm[4], rr[4];
    ln_total((LAS const float*)(L + L_PART + 2048), lane, mean, rstd);
    gather4(mean, mm); gather4(rstd, rr);
#pragma unroll
    for (int t = 0; t < 4; ++t) {
      v4u o; s[t] = 0.f; sq[t] = 0.f;
#pragma unroll
      for (int i = 0; i < 4; ++i) {
        const float c0 = rb(1.f + lo(mcb[i])), c1 = rb(1.f + hi(mcb[i]));
        o[i] = pk((x[t][2 * i] - mm[t]) * rr[t] * c0 + lo(msb[i]), (x[t][2 * i + 1] - mm[t]) * rr[t] * c1 + hi(msb[i]));
        const float a = lo(o[i]), c = hi(o[i]);
        s[t] += a + c; sq[t] += a * a + c * c;
      }
      *(LAS v4u*)(L + L_X + (t >> 1) * 16384 + (8 * w + g8) * 256 + ((t & 1) * 8 + tg) * 16) = o;
    }
    ((LAS float*)(L + L_PART + 4096))[w * 64 + lane] = ln_wave_reduce(s, sq, lane);
    BAR();
  }
  STAMP(8);
  v2u resh[2], resx[2];
  {
    f32x4 acc[2] = {{0.f, 0.f, 0.f, 0.f}, {0.f, 0.f, 0.f, 0.f}};
#pragma unroll
    for (int i = 0; i < 16; ++i)
#pragma unroll
      for (int tt = 0; tt < 2; ++tt) acc[tt] = mfma(wA[i], *(LAS const v4u*)(L + L_X + tt * 16384 + i * 1024 + lane * 16), acc[tt]);
    float mean, rstd;
    ln_total((LAS const float*)(L + L_PART + 4096), lane, mean, rstd);
    unsigned d[2][2];
#pragma unroll
    for (int tt = 0; tt < 2; ++tt) {
      const int src = 16 * (2 * tt + (c16 >> 3)) + (c16 & 7);
      const float mm = __shfl(mean, src), rr = __shfl(rstd, src);
      float o[4];
#pragma unroll
      for (int t = 0; t < 4; ++t) o[t] = gelu(rr * (acc[tt][t] - mm * cs1[t]));
      d[tt][0] = pk(o[0], o[1]); d[tt][1] = pk(o[2], o[3]);
      const int tok = 16 * tt + c16, sg = 8 * ((tok >> 1) & 3) + 2 * (tok >> 3) + (tok & 1);
      resh[tt] = *(LAS const v2u*)(L + L_X + (sg >> 4) * 16384 + (16 * g + 2 * w + (qq >> 1)) * 256 + (sg & 15) * 16 + (qq & 1) * 8);
      resx[tt] = *(LAS const v2u*)(L + L_X1 + (sg >> 4) * 4096 + (2 * w + (qq >> 1)) * 256 + (sg & 15) * 16 + (qq & 1) * 8);
    }
    swap16u(d[0][0], d[1][0]); swap16u(d[0][1], d[1][1]);
    const int sg = 16 * (qq & 1) + c16, tok = 32 * T + 8 * ((sg & 7) >> 1) + (sg & 1) + 2 * (sg >> 3);
    st128s(v4u{d[0][0], d[0][1], d[1][0], d[1][1]}, ws, OFF_HH + (tok >> 4) * 16384 + (16 * g + 2 * w + (qq >> 1)) * 256 + (tok & 15) * 16);
  }
  DRAIN();
  BAR();
  if (tid == 0) st32s(tag, ws, OFF_F2 + me * 128);
  STAMP(9);
  {
#pragma unroll
    for (int i = 0; i < 7; ++i) {
      const int n = i * 64 + lane, pos = n / 14, pc = n % 14;
      const int k = 8 * (2 * (pos >> 4) + ((pos >> 2) & 1)) + 4 * ((pos >> 3) & 1) + (pos & 3);
      const char* src = (const char*)p.w3 + (64 * w + k) * 3584 + m * 224 + pc * 16;
      glds16(src, (unsigned)(size_t)(L + L_W3) + 2 * w * 7168 + i * 1024);
      glds16(src + 32 * 3584, (unsigned)(size_t)(L + L_W3) + (2 * w + 1) * 7168 + i * 1024);
    }
  }
  if (w == 0) {
    const int j = lane & 7, dT = j < 4 ? 0 : ((j & 1) ? 1 : -1), Tn = min(max(T + dT, 0), 63);
    poll1(ws, OFF_F2 + (Tn * 4 + (j & 3)) * 128, tag);
  }
  BAR();
  STAMP(10);
  {
    const int gk = w >> 1, shift = gk == 0 ? -1 : gk == 1 ? 1 : gk == 2 ? -2 : 2;
    v4u xv[2][2];
#pragma unroll
    for (int tt = 0; tt < 2; ++tt) {
      const int tok = 32 * T + 16 * tt + c16 + shift;
      const unsigned o = (tok >= 0 && tok < 2048) ? OFF_HH + (tok >> 4) * 16384 + 2 * w * 1024 + qq * 256 + (tok & 15) * 16 : OOB;
      xv[tt][0] = ld128(ws, o, 1); xv[tt][1] = ld128(ws, o + 1024, 1);
    }
    const v2u gbv = ld64s(ws, OFF_MOD + 5120 + (128 * g + 16 * w + 4 * qq) * 2);
#pragma unroll
    for (int tt = 0; tt < 2; ++tt)
#pragma unroll
      for (int i = 0; i < 2; ++i) *(LAS v4u*)(L + L_X + tt * 16384 + (2 * w + i) * 1024 + lane * 16) = xv[tt][i];
    BAR();
    f32x4 acc[2] = {{0.f, 0.f, 0.f, 0.f}, {0.f, 0.f, 0.f, 0.f}};
#pragma unroll
    for (int i = 0; i < 16; ++i)
#pragma unroll
      for (int tt = 0; tt < 2; ++tt) acc[tt] = mfma(wB[i], *(LAS const v4u*)(L + L_X + tt * 16384 + i * 1024 + lane * 16), acc[tt]);
    unsigned d[2][2];
    f32x4 r[2];
#pragma unroll
    for (int tt = 0; tt < 2; ++tt) {
      d[tt][0] = pk(lo(resh[tt].x) + acc[tt][0], hi(resh[tt].x) + acc[tt][1]);
      d[tt][1] = pk(lo(resh[tt].y) + acc[tt][2], hi(resh[tt].y) + acc[tt][3]);
      r[tt] = f32x4{lo(resx[tt].x) + lo(gbv.x) * lo(d[tt][0]), hi(resx[tt].x) + hi(gbv.x) * hi(d[tt][0]),
                    lo(resx[tt].y) + lo(gbv.y) * lo(d[tt][1]), hi(resx[tt].y) + hi(gbv.y) * hi(d[tt][1])};
    }
    swap16u(d[0][0], d[1][0]); swap16u(d[0][1], d[1][1]);
    st128s(v4u{d[0][0], d[0][1], d[1][0], d[1][1]}, ws, OFF_X1P + (2 * T + (qq & 1)) * 16384 + (16 * g + 2 * w + (qq >> 1)) * 256 + c16 * 16);
    DRAIN();
    BAR();
    if (tid == 0) st32s(tag, ws, OFF_F3 + me * 128);
    STAMP(11);
#pragma unroll
    for (int tt = 0; tt < 2; ++tt) st128s(__builtin_bit_cast(v4u, r[tt]), ws, OFF_R + ((2 * T + tt) * 32 + 8 * g + w) * 1024 + lane * 16);
  }
  const int th = m >> 3, cj = m & 7;
  v4u w4[4][7];
#pragma unroll
  for (int n = 0; n < 2; ++n)
#pragma unroll
    for (int i = 0; i < 7; ++i) w4[n][i] = ld128(ws, OFF_W4P + ((4 * cj + n) * 56 + 7 * w + i) * 1024 + lane * 16, 1);
  SCHED();
  LAS char* w3a = L + L_W3 + (16 * (qq >> 1) + 4 * (qq & 1) + (c16 >> 2)) * 224 + (lane & 3) * 8;
  if (w < 7) {
    f32x4 cs = {0.f, 0.f, 0.f, 0.f};
#pragma unroll
    for (int i = 0; i < 16; ++i) {
      const v2u t0 = trread(w3a + i * 7168 + w * 32), t1 = trread(w3a + i * 7168 + 1792 + w * 32);
      cs = mfma(v4u{t0.x, t0.y, t1.x, t1.y}, ones, cs);
    }
    if (c16 == 0) *(LAS f32x4*)(L + L_CS3 + w * 64 + qq * 16) = cs;
  }
  if (w == 7) poll1(ws, OFF_F3 + (16 * squad + c16) * 128, tag);
  BAR();
  STAMP(12);
  {
    v4u xv[16];
#pragma unroll
    for (int i = 0; i < 16; ++i) xv[i] = ld128(ws, OFF_X1P + (8 * squad + w) * 16384 + i * 1024 + lane * 16, 1);
#pragma unroll
    for (int n = 2; n < 4; ++n)
#pragma unroll
      for (int i = 0; i < 7; ++i) w4[n][i] = ld128(ws, OFF_W4P + ((4 * cj + n) * 56 + 7 * w + i) * 1024 + lane * 16, 1);
    SCHED();
    f32x4 acc[7], aS = {0.f, 0.f, 0.f, 0.f}, aQ = {0.f, 0.f, 0.f, 0.f};
#pragma unroll
    for (int n = 0; n < 7; ++n) acc[n] = f32x4{0.f, 0.f, 0.f, 0.f};
#pragma unroll
    for (int i = 0; i < 16; ++i) {
      aS = mfma(ones, xv[i], aS);
      aQ = mfma(xv[i], xv[i], aQ);
#pragma unroll
      for (int n = 0; n < 7; ++n) {
        const v2u t0 = trread(w3a + i * 7168 + n * 32), t1 = trread(w3a + i * 7168 + 1792 + n * 32);
        acc[n] = mfma(v4u{t0.x, t0.y, t1.x, t1.y}, xv[i], acc[n]);
      }
    }
    STAMP(13);
    const int r3 = c16 & 3;
    const float dq = r3 == 0 ? aQ[0] : r3 == 1 ? aQ[1] : r3 == 2 ? aQ[2] : aQ[3];
    float mean, rstd;
    ln_finish(aS[0], __shfl(dq, (c16 >> 2) * 16 + c16), mean, rstd);
    unsigned d[7][2];
#pragma unroll
    for (int n = 0; n < 7; ++n) {
      const f32x4 cs = *(LAS const f32x4*)(L + L_CS3 + n * 64 + qq * 16);
      float o[4];
#pragma unroll
      for (int t = 0; t < 4; ++t) o[t] = gelu(rstd * (acc[n][t] - mean * cs[t]));
      d[n][0] = pk(o[0], o[1]); d[n][1] = pk(o[2], o[3]);
    }
    const unsigned go = OFF_G + (8 * squad + w) * 57344 + m * 3584;
#pragma unroll
    for (int j = 0; j < 3; ++j) st128s(v4u{d[2 * j][0], d[2 * j][1], d[2 * j + 1][0], d[2 * j + 1][1]}, ws, go + j * 1024 + lane * 16);
    unsigned x0 = d[6][0], y0 = d[6][0], x1 = d[6][1], y1 = d[6][1];
    swap16u(x0, y0); swap16u(x1, y1);
    st128s(v4u{x0, x1, y0, y1}, ws, (qq & 1) ? OOB : go + 3072 + (qq >> 1) * 256 + c16 * 16);
  }
  DRAIN();
  BAR();
  if (tid == 0) st32s(tag, ws, OFF_F4 + me * 128);
  STAMP(14);
  if (w == 0) poll1(ws, OFF_F4 + (16 * squad + c16) * 128, tag);
  BAR();
  STAMP(15);
  {
    v4u xv[4][7];
#pragma unroll
    for (int tt = 0; tt < 4; ++tt)
#pragma unroll
      for (int i = 0; i < 7; ++i) xv[tt][i] = ld128(ws, OFF_G + (8 * squad + 4 * th + tt) * 57344 + (7 * w + i) * 1024 + lane * 16, 1);
    const int ett = w >> 1, en = 2 * (w & 1);
    const unsigned ro = OFF_R + ((8 * squad + 4 * th + ett) * 32 + 4 * cj + en) * 1024 + lane * 16;
    v4u r0, r1; v2u g0, g1;
    SCHED();
#pragma unroll
    for (int tt = 0; tt < 4; ++tt) {
      f32x4 acc[4];
#pragma unroll
      for (int n = 0; n < 4; ++n) acc[n] = f32x4{0.f, 0.f, 0.f, 0.f};
#pragma unroll
      for (int i = 0; i < 7; ++i)
#pragma unroll
        for (int n = 0; n < 4; ++n) acc[n] = mfma(w4[n][i], xv[tt][i], acc[n]);
#pragma unroll
      for (int n = 0; n < 4; ++n) *(LAS f32x4*)(L + ((w * 4 + tt) * 4 + n) * 1024 + lane * 16) = acc[n];
      SCHED();
      if (tt == 0) {
        r0 = ld128(ws, ro, 1); r1 = ld128(ws, ro + 1024, 1);
        g0 = ld64s(ws, OFF_MOD + 5120 + (64 * cj + 16 * en + 4 * qq) * 2); g1 = ld64s(ws, OFF_MOD + 5120 + (64 * cj + 16 * en + 16 + 4 * qq) * 2);
        SCHED();
      }
    }
    BAR();
    STAMP(16);
    f32x4 s0 = {0.f, 0.f, 0.f, 0.f}, s1 = {0.f, 0.f, 0.f, 0.f};
#pragma unroll
    for (int v = 0; v < 8; ++v) {
      s0 += *(LAS const f32x4*)(L + ((v * 4 + ett) * 4 + en) * 1024 + lane * 16);
      s1 += *(LAS const f32x4*)(L + ((v * 4 + ett) * 4 + en + 1) * 1024 + lane * 16);
    }
    const f32x4 q0 = __builtin_bit_cast(f32x4, r0), q1 = __builtin_bit_cast(f32x4, r1);
    u16* op = p.out + (128 * squad + 64 * th + 16 * ett + c16) * 512 + 64 * cj + 16 * en + 4 * qq;
    *(GAS v2u*)(op) = v2u{pk(q0[0] + lo(g0.x) * s0[0], q0[1] + hi(g0.x) * s0[1]), pk(q0[2] + lo(g0.y) * s0[2], q0[3] + hi(g0.y) * s0[3])};
    *(GAS v2u*)(op + 16) = v2u{pk(q1[0] + lo(g1.x) * s1[0], q1[1] + hi(g1.x) * s1[1]), pk(q1[2] + lo(g1.y) * s1[2], q1[3] + hi(g1.y) * s1[3])};
  }
  if (b == 0 && tid == 0) st32s(tag, ws, OFF_EPOCH);
  STAMP(17);
}
}

extern "C" void kernel_launch(void* const* d_in, const int* in_sizes, int n_in, void* d_out, int out_size, void* d_ws,
                              size_t ws_size, hipStream_t stream) {
  mk::Params p;
  p.q = (const mk::u16*)d_in[0]; p.k = (const mk::u16*)d_in[1]; p.v = (const mk::u16*)d_in[2];
  p.sk = (const mk::u16*)d_in[3]; p.ssmk = (const mk::u16*)d_in[4]; p.ssmD = (const mk::u16*)d_in[5];
  p.w1 = (const mk::u16*)d_in[7]; p.w2 = (const mk::u16*)d_in[8]; p.w3 = (const mk::u16*)d_in[9]; p.w4 = (const mk::u16*)d_in[10];
  p.c = (const mk::u16*)d_in[11]; p.wmod = (const mk::u16*)d_in[12];
  p.out = (mk::u16*)d_out; p.ws = (char*)d_ws;
  mk::mega<<<256, 512, 0, stream>>>(p);
}
```

```cpp
#define DIAG 0
#include <hip/hip_runtime.h>
#include <stdint.h>

namespace mk {
typedef unsigned short u16;
typedef unsigned v4u __attribute__((ext_vector_type(4)));
typedef unsigned v2u __attribute__((ext_vector_type(2)));
typedef float f32x4 __attribute__((ext_vector_type(4)));
typedef float f32x2 __attribute__((ext_vector_type(2)));
typedef __bf16 bf16x8 __attribute__((ext_vector_type(8)));
typedef __bf16 bf16x2 __attribute__((ext_vector_type(2)));
typedef short s16x4 __attribute__((ext_vector_type(4)));
typedef __amdgpu_buffer_rsrc_t rsrc_t;
#define LAS __attribute__((address_space(3)))
#define GAS __attribute__((address_space(1)))
#define DEVI __device__ __forceinline__

constexpr int SC1 = 16;
constexpr int OFF_EPOCH = 0;
constexpr int OFF_F0 = 4096;
constexpr int OFF_FR = OFF_F0 + 1024 * 128;
constexpr int OFF_F1 = OFF_FR + 8 * 8192;
constexpr int OFF_F2 = OFF_F1 + 8 * 8192;
constexpr int OFF_F3 = OFF_F2 + 256 * 128;
constexpr int OFF_F4 = OFF_F3 + 256 * 128;
constexpr int OFF_MOD = OFF_F4 + 256 * 128;
constexpr int OFF_UR = 1 << 20;
constexpr int OFF_Z = OFF_UR + (2 << 20);
constexpr int OFF_W1P = OFF_Z + (2 << 20);
constexpr int OFF_W2P = OFF_W1P + (512 << 10);
constexpr int OFF_W4P = OFF_W2P + (512 << 10);
constexpr int OFF_HH = OFF_W4P + (2 << 20);
constexpr int OFF_X1P = OFF_HH + (2 << 20);
constexpr int OFF_R = OFF_X1P + (2 << 20);
constexpr int OFF_G = OFF_R + (4 << 20);
constexpr int WS_USED = OFF_G + 128 * 57344;
constexpr unsigned OOB = 0x80000000u;
constexpr int L_W3 = 0;
constexpr int L_KC = 112256;
constexpr int L_UC = L_KC + 8 * 4160;
constexpr int L_X = 114688;
constexpr int L_X1 = L_X + 32768;
constexpr int L_PART = L_X1 + 8192;
constexpr int L_CS3 = L_PART + 3 * 2048;
constexpr int L_CNT = 106496;
static_assert(L_UC + 4 * 4576 == 163840, "lds");
static_assert(L_CS3 + 448 <= 163840, "lds");

#define STAMP(i) do { if ((i) == 0) dg0 = __builtin_amdgcn_s_memrealtime(); if ((i) == DIAG) dg1 = __builtin_amdgcn_s_memrealtime(); } while (0)

struct Params {
  const u16 *q, *k, *v, *sk, *ssmk, *ssmD, *w1, *w2, *w3, *w4, *c, *wmod;
  u16* out;
  char* ws;
};

DEVI float lo(unsigned u) { return __builtin_bit_cast(float, u << 16); }
DEVI float hi(unsigned u) { return __builtin_bit_cast(float, u & 0xffff0000u); }
DEVI unsigned pk(float a, float b) { f32x2 f = {a, b}; return __builtin_bit_cast(unsigned, __builtin_convertvector(f, bf16x2)); }
DEVI float rb(float a) { return lo(pk(a, a)); }
DEVI f32x4 mfma(v4u a, v4u b, f32x4 c) {
  return __builtin_amdgcn_mfma_f32_16x16x32_bf16(__builtin_bit_cast(bf16x8, a), __builtin_bit_cast(bf16x8, b), c, 0, 0, 0);
}
DEVI rsrc_t mkrs(const void* p, int bytes) { return __builtin_amdgcn_make_buffer_rsrc((void*)p, 0, bytes, 0x00020000); }
DEVI v4u ld128(rsrc_t r, unsigned off, int aux) { return aux ? __builtin_amdgcn_raw_buffer_load_b128(r, off, 0, SC1) : __builtin_amdgcn_raw_buffer_load_b128(r, off, 0, 0); }
DEVI v2u ld64s(rsrc_t r, unsigned off) { return __builtin_amdgcn_raw_buffer_load_b64(r, off, 0, SC1); }
DEVI unsigned ld32(rsrc_t r, unsigned off, int aux) { return aux ? __builtin_amdgcn_raw_buffer_load_b32(r, off, 0, SC1) : __builtin_amdgcn_raw_buffer_load_b32(r, off, 0, 0); }
DEVI void st128s(v4u v, rsrc_t r, unsigned off) { __builtin_amdgcn_raw_buffer_store_b128(v, r, off, 0, SC1); }
DEVI void st32s(unsigned v, rsrc_t r, unsigned off) { __builtin_amdgcn_raw_buffer_store_b32(v, r, off, 0, SC1); }
#define MEMFENCE() asm volatile("" ::: "memory")
#define DRAIN() asm volatile("s_waitcnt vmcnt(0)" ::: "memory")
#define BAR() asm volatile("s_waitcnt lgkmcnt(0)\n\ts_barrier" ::: "memory")
#define SCHED() __builtin_amdgcn_sched_barrier(0)
constexpr unsigned kSpinCap = 1u << 21;

DEVI void poll1(rsrc_t r, unsigned off, unsigned tag) {
  for (unsigned s = 0; s < kSpinCap; ++s) {
    MEMFENCE();
    const unsigned f = ld32(r, off, 1);
    if (__all(f == tag)) break;
  }
  MEMFENCE();
}
DEVI void poll4(rsrc_t r, unsigned off, unsigned tag) {
  for (unsigned s = 0; s < kSpinCap; ++s) {
    MEMFENCE();
    const v4u f = ld128(r, off, 1);
    if (__all((f.x == tag) & (f.y == tag) & (f.z == tag) & (f.w == tag))) break;
  }
  MEMFENCE();
}
DEVI float fbits(unsigned u) { return __builtin_bit_cast(float, u); }
DEVI unsigned ubits(float f) { return __builtin_bit_cast(unsigned, f); }
DEVI float ror8(float x) { return fbits(__builtin_amdgcn_update_dpp(0u, ubits(x), 0x128, 0xf, 0xf, false)); }
DEVI void swap32(float& a, float& b) { auto r = __builtin_amdgcn_permlane32_swap(ubits(a), ubits(b), false, false); a = fbits(r[0]); b = fbits(r[1]); }
DEVI void swap16(float& a, float& b) { auto r = __builtin_amdgcn_permlane16_swap(ubits(a), ubits(b), false, false); a = fbits(r[0]); b = fbits(r[1]); }
DEVI void swap16u(unsigned& a, unsigned& b) { auto r = __builtin_amdgcn_permlane16_swap(a, b, false, false); a = r[0]; b = r[1]; }

DEVI float ln_wave_reduce(const float (&s)[4], const float (&qv)[4], int lane) {
  float a0 = s[0], b0 = s[2], a1 = s[1], b1 = s[3], c0 = qv[0], d0 = qv[2], c1 = qv[1], d1 = qv[3];
  swap32(a0, b0); swap32(a1, b1); swap32(c0, d0); swap32(c1, d1);
  float s0 = a0 + b0, s1 = a1 + b1, q0 = c0 + d0, q1 = c1 + d1;
  swap16(s0, s1); swap16(q0, q1);
  const float x = s0 + s1, y = q0 + q1;
  const bool b3 = lane & 8;
  return (b3 ? y : x) + ror8(b3 ? x : y);
}
DEVI void ln_finish(float sum, float sq, float& mean, float& rstd) {
  const float m = sum * (1.f / 512.f);
  const float var = fmaxf(sq * (1.f / 512.f) - m * m, 0.f);
  mean = rb(m);
  rstd = __builtin_amdgcn_rcpf(rb(__builtin_amdgcn_sqrtf(rb(rb(var) + 1e-5f))));
}
DEVI void ln_total(LAS const float* part, int lane, float& mean, float& rstd) {
  float t = 0.f;
#pragma unroll
  for (int w = 0; w < 8; ++w) t += part[w * 64 + lane];
  const float o = ror8(t);
  const bool b3 = lane & 8;
  ln_finish(b3 ? o : t, b3 ? t : o, mean, rstd);
}
DEVI void gather4(float x, float (&o)[4]) {
  float e = x, d = x;
  swap16(e, d);
  float e2 = e, d2 = d;
  swap32(e, e2); swap32(d, d2);
  o[0] = e; o[2] = e2; o[1] = d; o[3] = d2;
}
DEVI float gelu(float x) {
  const float t = x * (2.302208198f + 0.1029432397f * x * x);
  return x * __builtin_amdgcn_rcpf(1.f + __builtin_amdgcn_exp2f(-t));
}
DEVI void glds16(const void* gsrc, unsigned lds_dst) {
  unsigned keep;
  asm volatile("s_mov_b32 %0, m0\n\ts_mov_b32 m0, %2\n\ts_nop 0\n\tglobal_load_lds_dwordx4 %1, off\n\ts_mov_b32 m0, %0" : "=&s"(keep) : "v"(gsrc), "s"(lds_dst) : "memory");
}
DEVI v2u trread(LAS char* a) { return __builtin_bit_cast(v2u, __builtin_amdgcn_ds_read_tr16_b64_v4i16((LAS s16x4*)a)); }

__global__ void __launch_bounds__(512, 2) mega(Params p) {
  __shared__ __attribute__((aligned(16))) char smem[163840];
  LAS char* L = (LAS char*)smem;
  const int tid = threadIdx.x, lane = tid & 63, w = __builtin_amdgcn_readfirstlane(tid >> 6);
  const int b = blockIdx.x, team = b & 7, rank = b >> 3, me = team * 32 + rank;
  const int c16 = lane & 15, qq = lane >> 4;
  unsigned long long dg0 = 0, dg1 = 0;
  STAMP(0);
  const rsrc_t ws = mkrs(p.ws, WS_USED);
  if (tid == 0) *(LAS unsigned*)(L + L_CNT) = 0u;
  const unsigned epoch = ld32(ws, OFF_EPOCH, 1);
  BAR();

  const rsrc_t rssm = mkrs(p.ssmk, 512 * 2048 * 2);
  v4u kp0, kp1;
  if (w < 4) {
    const rsrc_t rk = mkrs(p.k, 2048 * 1024), rv = mkrs(p.v, 2048 * 1024);
    const int tg = lane & 7, d0 = 64 * team + 2 * (8 * w + (lane >> 3)), s0 = 64 * rank + 8 * tg;
    unsigned kk[11], vv[8];
#pragma unroll
    for (int i = 0; i < 3; ++i) kk[i] = ld32(rk, s0 ? (unsigned)((s0 - 3) * 1024 + d0 * 2) + i * 1024 : OOB, 0);
#pragma unroll
    for (int i = 3; i < 11; ++i) kk[i] = ld32(rk, (unsigned)(s0 * 1024 + d0 * 2) + (i - 3) * 1024, 0);
#pragma unroll
    for (int i = 0; i < 8; ++i) vv[i] = ld32(rv, (unsigned)((s0 + i) * 1024 + d0 * 2), 0);
    const v4u sk = *(const GAS v4u*)(p.sk + d0 * 4);
    {
      const int ch = tid >> 8, a = tid & 255;
      const unsigned o = (unsigned)((2 * me + ch) * 4096 + a * 16);
      kp1 = ld128(rssm, o, 0);
      kp0 = ld128(rssm, a ? o - 16 : OOB, 0);
    }
    const float a0 = lo(sk.x), a1 = hi(sk.x), a2 = lo(sk.y), a3 = hi(sk.y), b0 = lo(sk.z), b1 = hi(sk.z), b2 = lo(sk.w), b3 = hi(sk.w);
    float x0[8], x1[8];
#pragma unroll
    for (int t = 0; t < 8; ++t) {
      x0[t] = (lo(kk[t]) * a3 + lo(kk[t + 1]) * a2 + lo(kk[t + 2]) * a1 + lo(kk[t + 3]) * a0) * lo(vv[t]);
      x1[t] = (hi(kk[t]) * b3 + hi(kk[t + 1]) * b2 + hi(kk[t + 2]) * b1 + hi(kk[t + 3]) * b0) * hi(vv[t]);
    }
    const v4u o0 = {pk(x0[7], x0[6]), pk(x0[5], x0[4]), pk(x0[3], x0[2]), pk(x0[1], x0[0])};
    const v4u o1 = {pk(x1[7], x1[6]), pk(x1[5], x1[4]), pk(x1[3], x1[2]), pk(x1[1], x1[0])};
    const unsigned uo = OFF_UR + d0 * 4096 + (2040 - s0) * 2;
    st128s(o0, ws, uo);
    st128s(o1, ws, uo + 4096);
    DRAIN();
    const unsigned tag0 = epoch * 0x9E3779B1u + 0x7F4A7C15u;
    if (lane == 0) st32s(tag0, ws, OFF_F0 + ((team * 4 + w) * 32 + rank) * 128);
    STAMP(1);
  } else {
    {
      const int ch = tid >> 8, a = tid & 255;
      const unsigned o = (unsigned)((2 * me + ch) * 4096 + a * 16);
      kp1 = ld128(rssm, o, 0);
      kp0 = ld128(rssm, a ? o - 16 : OOB, 0);
    }
    if (w == 4) {
      if (rank < 24) {
        const int n0 = 384 * team + 16 * rank + 8 * (lane & 1), rg = lane >> 1;
        v4u wv[16];
#pragma unroll
        for (int i = 0; i < 16; ++i) wv[i] = *(const GAS v4u*)(p.wmod + (16 * rg + i) * 3072 + n0);
        const v4u c0 = *(const GAS v4u*)(p.c + 16 * rg), c1 = *(const GAS v4u*)(p.c + 16 * rg + 8);
        float acc[8] = {0.f, 0.f, 0.f, 0.f, 0.f, 0.f, 0.f, 0.f};
#pragma unroll
        for (int i = 0; i < 16; ++i) {
          const unsigned cu = i < 8 ? c0[i >> 1] : c1[(i - 8) >> 1];
          const float x = (i & 1) ? hi(cu) : lo(cu);
          const float s = rb(x * __builtin_amdgcn_rcpf(1.f + __builtin_amdgcn_exp2f(-1.442695041f * x)));
#pragma unroll
          for (int j = 0; j < 4; ++j) { acc[2 * j] += s * lo(wv[i][j]); acc[2 * j + 1] += s * hi(wv[i][j]); }
        }
#pragma unroll
        for (int o = 2; o < 64; o <<= 1)
#pragma unroll
          for (int j = 0; j < 8; ++j) acc[j] += __shfl_xor(acc[j], o);
        if (lane < 2) st128s(v4u{pk(acc[0], acc[1]), pk(acc[2], acc[3]), pk(acc[4], acc[5]), pk(acc[6], acc[7])}, ws, OFF_MOD + n0 * 2);
      }
    } else if (w < 7) {
      const int u = (w - 5) * 256 + b;
      if (u < 352) {
        const u16* src; int cb, ks; unsigned dst; int k0, k1;
        if (u < 128) {
          src = u < 64 ? p.w1 : p.w2; cb = (u >> 4) & 3; ks = u & 15;
          dst = (u < 64 ? OFF_W1P : OFF_W2P) + (cb * 8 * 16 + ks) * 1024;
          k0 = 32 * ks + 8 * qq; k1 = k0 + 4;
        } else {
          const int v = u - 128; src = p.w4; cb = v / 56; ks = v % 56;
          dst = OFF_W4P + (cb * 8 * 56 + ks) * 1024;
          const int P = 4 * ks + qq, sl = P / 14, pp = P % 14;
          if (pp < 12) { k0 = 112 * sl + 32 * (pp >> 2) + 4 * (pp & 3); k1 = k0 + 16; }
          else { k0 = 112 * sl + 96 + 8 * (pp - 12); k1 = k0 + 4; }
        }
        const int nstride = u < 128 ? 16 * 1024 : 56 * 1024;
        v4u raw[8];
#pragma unroll
        for (int j = 0; j < 8; ++j) raw[j] = *(const GAS v4u*)(src + ((j < 4 ? k0 + j : k1 + j - 4) * 512 + 128 * cb + 8 * c16));
        LAS char* stg = L + (w - 5) * 9216;
#pragma unroll
        for (int j = 0; j < 8; ++j) *(LAS v4u*)(stg + (qq * 8 + j) * 288 + c16 * 16) = raw[j];
        asm volatile("s_waitcnt lgkmcnt(0)" ::: "memory");
#pragma unroll
        for (int i = 0; i < 8; ++i) {
          LAS char* a = stg + (qq * 8 + (c16 >> 2)) * 288 + i * 32 + (lane & 3) * 8;
          const v2u t0 = trread(a), t1 = trread(a + 4 * 288);
          st128s(v4u{t0.x, t0.y, t1.x, t1.y}, ws, dst + i * nstride + lane * 16);
        }
      }
    } else {
      unsigned x = ld32(ws, OFF_F0 + ((team * 4 + (rank >> 3)) * 32 + (lane & 31)) * 128, 1);
      x ^= ld32(ws, OFF_FR + team * 8192 + lane * 128, 1);
      x ^= ld32(ws, OFF_F1 + team * 8192 + lane * 128, 1);
      x ^= ld32(ws, OFF_F2 + (lane >> 4) * 32768 + ((me & ~15) + c16) * 128, 1);
      asm volatile("" ::"v"(x));
    }
    if (w < 7) {
      DRAIN();
      unsigned old = 0;
      if (lane == 0) old = __hip_atomic_fetch_add((LAS unsigned*)(L + L_CNT), 1u, __ATOMIC_RELAXED, __HIP_MEMORY_SCOPE_WORKGROUP);
      old = __builtin_amdgcn_readfirstlane(old);
      const unsigned tag0 = epoch * 0x9E3779B1u + 0x7F4A7C15u;
      if (old == 2 && lane < 8) st32s(tag0, ws, OFF_FR + lane * 8192 + (b >> 2) * 128 + (b & 3) * 4);
    }
  }
  const unsigned tag = __builtin_amdgcn_readfirstlane(epoch * 0x9E3779B1u + 0x7F4A7C15u);
  {
    const int ch = tid >> 8, a = tid & 255;
    LAS char* kc = L + L_KC + ch * 4 * 4160 + (a + 3) * 16;
    *(LAS v4u*)(kc) = v4u{kp0.y, kp0.z, kp0.w, kp1.x};
    *(LAS v4u*)(kc + 4160) = v4u{kp0.z, kp0.w, kp1.x, kp1.y};
    *(LAS v4u*)(kc + 2 * 4160) = v4u{kp0.w, kp1.x, kp1.y, kp1.z};
    *(LAS v4u*)(kc + 3 * 4160) = kp1;
    const v4u z4 = {0u, 0u, 0u, 0u};
    if (tid < 24) *(LAS v4u*)(L + L_KC + (tid / 3) * 4160 + (tid % 3) * 16) = z4;
    if (tid >= 128 && tid < 256 && (tid & 31) < 30) *(LAS v4u*)(L + L_UC + ((tid >> 5) & 3) * 4576 + (256 + (tid & 31)) * 16) = z4;
  }
  if (w == 0) poll1(ws, OFF_F0 + ((team * 4 + (rank >> 3)) * 32 + (lane & 31)) * 128, tag);
  BAR();
  STAMP(2);

  {
    const int ch = tid >> 8, a = tid & 255;
    const unsigned o = OFF_UR + (2 * me + ch) * 4096 + a * 16;
    const v4u u1 = ld128(ws, o, 1);
    const unsigned nx = ld32(ws, a < 255 ? o + 16 : OOB, 1);
    LAS char* uc = L + L_UC + ch * 2 * 4576 + a * 16;
    *(LAS v4u*)(uc + 4576) = u1;
    *(LAS v4u*)(uc) = v4u{__builtin_amdgcn_alignbit(u1.y, u1.x, 16), __builtin_amdgcn_alignbit(u1.z, u1.y, 16),
                          __builtin_amdgcn_alignbit(u1.w, u1.z, 16), __builtin_amdgcn_alignbit(nx, u1.w, 16)};
  }
  BAR();
  STAMP(3);
  f32x4 cacc[8];
  if (w < 4) {
    const int ch = w >> 1, t0 = 4 * (w & 1);
    LAS const char* pa = L + L_KC + (ch * 4 + (c16 & 3)) * 4160 + (qq + (c16 >> 2)) * 16 + t0 * 64;
    LAS const char* pb = L + L_UC + (ch * 2 + (c16 & 1)) * 4576 + (7 - (c16 >> 1)) * 64 + qq * 16 + t0 * 64;
#pragma unroll
    for (int g = 0; g < 8; ++g) cacc[g] = f32x4{0.f, 0.f, 0.f, 0.f};
    v4u fa[2][8], fb[2][8];
#pragma unroll
    for (int i = 0; i < 8; ++i) { fa[0][i] = *(LAS const v4u*)(pa + 512 * i); fb[0][i] = *(LAS const v4u*)(pb + 512 * (7 - i)); }
#pragma unroll
    for (int t = 0; t < 4; ++t) {
      if (t < 3) {
#pragma unroll
        for (int i = 0; i < 8; ++i) {
          fa[(t + 1) & 1][i] = *(LAS const v4u*)(pa + 64 * (t + 1) + 512 * i);
          fb[(t + 1) & 1][i] = *(LAS const v4u*)(pb + 64 * (t + 1) + 512 * (7 - i));
        }
      }
#pragma unroll
      for (int i = 0; i < 8; ++i)
#pragma unroll
        for (int n = 0; n + i < 8; ++n) cacc[i + n] = mfma(fa[t & 1][i], fb[t & 1][n], cacc[i + n]);
      if (t < 3) {
#pragma unroll
        for (int i = 0; i < 16; ++i) {
          __builtin_amdgcn_sched_group_barrier(0x100, 1, 0);
          __builtin_amdgcn_sched_group_barrier(0x8, 2, 0);
        }
        __builtin_amdgcn_sched_group_barrier(0x8, 4, 0);
      }
      SCHED();
    }
  }
  BAR();
  if (w < 4) {
#pragma unroll
    for (int g = 0; g < 8; ++g) *(LAS f32x4*)(L + L_KC + (w * 8 + g) * 1024 + lane * 16) = cacc[g];
  }
  BAR();
  STAMP(4);
  {
    const f32x4 y0 = *(LAS const f32x4*)(L + L_KC + (0 * 8 + w) * 1024 + lane * 16) + *(LAS const f32x4*)(L + L_KC + (1 * 8 + w) * 1024 + lane * 16);
    const f32x4 y1 = *(LAS const f32x4*)(L + L_KC + (2 * 8 + w) * 1024 + lane * 16) + *(LAS const f32x4*)(L + L_KC + (3 * 8 + w) * 1024 + lane * 16);
    const int a8 = 256 * w + 32 * (c16 >> 1) + 8 * qq;
    const v4u k0 = *(LAS const v4u*)(L + L_UC + 4576 + (2040 - a8) * 2), k1 = *(LAS const v4u*)(L + L_UC + 3 * 4576 + (2040 - a8) * 2);
    const unsigned dd = *(const GAS unsigned*)(p.ssmD + 2 * me);
    const float D0 = lo(dd), D1 = hi(dd);
    const int sh = (c16 & 1) ? 16 : 0;
    v4u o;
#pragma unroll
    for (int t = 0; t < 4; ++t) o[t] = pk(y0[t] + hi(k0[3 - t] << sh) * D0, y1[t] + hi(k1[3 - t] << sh) * D1);
    st128s(o, ws, OFF_Z + (8 * w + (c16 >> 1)) * 32768 + me * 128 + (2 * qq + (c16 & 1)) * 16);
  }
  DRAIN();
  BAR();
  if (tid < 8) st32s(tag, ws, OFF_F1 + tid * 8192 + (b >> 2) * 128 + (b & 3) * 4);
  STAMP(5);

  const int T = me >> 2, g = me & 3, m = me & 15, squad = me >> 4;
  const int g8 = lane >> 3, tg = lane & 7;
  if (w == 7) poll4(ws, OFF_FR + team * 8192 + lane * 128, tag);
  v4u qv[4];
#pragma unroll
  for (int t = 0; t < 4; ++t) qv[t] = *(const GAS v4u*)(p.q + (32 * T + 8 * (tg >> 1) + (tg & 1) + 2 * t) * 512 + 64 * w + 8 * g8);
  BAR();
  v4u wA[16];
  const unsigned mo = OFF_MOD + (64 * w + 8 * g8) * 2;
  const v4u msa = ld128(ws, mo, 1), mca = ld128(ws, mo + 1024, 1), mga = ld128(ws, mo + 2048, 1), msb = ld128(ws, mo + 3072, 1), mcb = ld128(ws, mo + 4096, 1);
#pragma unroll
  for (int i = 0; i < 16; ++i) wA[i] = ld128(ws, OFF_W1P + ((8 * g + w) * 16 + i) * 1024 + lane * 16, 1);
  SCHED();
  float gh[4][8];
  {
    float s[4], sq[4];
#pragma unroll
    for (int t = 0; t < 4; ++t) {
      s[t] = 0.f; sq[t] = 0.f;
#pragma unroll
      for (int i = 0; i < 4; ++i) { const float a = lo(qv[t][i]), c = hi(qv[t][i]); s[t] += a + c; sq[t] += a * a + c * c; }
    }
    ((LAS float*)(L + L_PART))[w * 64 + lane] = ln_wave_reduce(s, sq, lane);
    BAR();
    float mean, rstd, mm[4], rr[4];
    ln_total((LAS const float*)(L + L_PART), lane, mean, rstd);
    gather4(mean, mm); gather4(rstd, rr);
#pragma unroll
    for (int i = 0; i < 4; ++i) {
      const float c0 = rb(1.f + lo(mca[i])), c1 = rb(1.f + hi(mca[i]));
#pragma unroll
      for (int t = 0; t < 4; ++t) {
        gh[t][2 * i] = lo(mga[i]) * ((lo(qv[t][i]) - mm[t]) * rr[t] * c0 + lo(msa[i]));
        gh[t][2 * i + 1] = hi(mga[i]) * ((hi(qv[t][i]) - mm[t]) * rr[t] * c1 + hi(msa[i]));
      }
    }
  }
  const v4u ones = {0x3F803F80u, 0x3F803F80u, 0x3F803F80u, 0x3F803F80u};
  f32x4 cs1 = {0.f, 0.f, 0.f, 0.f};
#pragma unroll
  for (int i = 0; i < 16; ++i) cs1 = mfma(wA[i], ones, cs1);
  STAMP(6);
  if (w == 0) poll4(ws, OFF_F1 + team * 8192 + lane * 128, tag);
  BAR();
  STAMP(7);
  v4u wB[16];
  {
    v4u zz[4];
#pragma unroll
    for (int i = 0; i < 4; ++i) zz[i] = ld128(ws, OFF_Z + T * 32768 + (32 * w + 4 * g8 + i) * 128 + tg * 16, 1);
#pragma unroll
    for (int i = 0; i < 16; ++i) wB[i] = ld128(ws, OFF_W2P + ((8 * g + w) * 16 + i) * 1024 + lane * 16, 1);
    SCHED();
    float x[4][8], s[4], sq[4];
#pragma unroll
    for (int t = 0; t < 4; ++t) {
      s[t] = 0.f; sq[t] = 0.f;
#pragma unroll
      for (int i = 0; i < 4; ++i) {
        const float a = lo(qv[t][i]) + gh[t][2 * i] * lo(zz[i][t]), c = hi(qv[t][i]) + gh[t][2 * i + 1] * hi(zz[i][t]);
        x[t][2 * i] = a; x[t][2 * i + 1] = c;
        s[t] += a + c; sq[t] += a * a + c * c;
      }
    }
    ((LAS float*)(L + L_PART + 2048))[w * 64 + lane] = ln_wave_reduce(s, sq, lane);
    if ((w >> 1) == g) {
#pragma unroll
      for (int t = 0; t < 4; ++t)
        *(LAS v4u*)(L + L_X1 + (t >> 1) * 4096 + ((w & 1) * 8 + g8) * 256 + ((t & 1) * 8 + tg) * 16) =
            v4u{pk(x[t][0], x[t][1]), pk(x[t][2], x[t][3]), pk(x[t][4], x[t][5]), pk(x[t][6], x[t][7])};
    }
    BAR();
    float mean, rstd, mm[4], rr[4];
    ln_total((LAS const float*)(L + L_PART + 2048), lane, mean, rstd);
    gather4(mean, mm); gather4(rstd, rr);
#pragma unroll
    for (int t = 0; t < 4; ++t) {
      v4u o; s[t] = 0.f; sq[t] = 0.f;
#pragma unroll
      for (int i = 0; i < 4; ++i) {
        const float c0 = rb(1.f + lo(mcb[i])), c1 = rb(1.f + hi(mcb[i]));
        o[i] = pk((x[t][2 * i] - mm[t]) * rr[t] * c0 + lo(msb[i]), (x[t][2 * i + 1] - mm[t]) * rr[t] * c1 + hi(msb[i]));
        const float a = lo(o[i]), c = hi(o[i]);
        s[t] += a + c; sq[t] += a * a + c * c;
      }
      *(LAS v4u*)(L + L_X + (t >> 1) * 16384 + (8 * w + g8) * 256 + ((t & 1) * 8 + tg) * 16) = o;
    }
    ((LAS float*)(L + L_PART + 4096))[w * 64 + lane] = ln_wave_reduce(s, sq, lane);
    BAR();
  }
  STAMP(8);
  v2u resh[2], resx[2];
  {
    f32x4 acc[2] = {{0.f, 0.f, 0.f, 0.f}, {0.f, 0.f, 0.f, 0.f}};
#pragma unroll
    for (int i = 0; i < 16; ++i)
#pragma unroll
      for (int tt = 0; tt < 2; ++tt) acc[tt] = mfma(wA[i], *(LAS const v4u*)(L + L_X + tt * 16384 + i * 1024 + lane * 16), acc[tt]);
    float mean, rstd;
    ln_total((LAS const float*)(L + L_PART + 4096), lane, mean, rstd);
    unsigned d[2][2];
#pragma unroll
    for (int tt = 0; tt < 2; ++tt) {
      const int src = 16 * (2 * tt + (c16 >> 3)) + (c16 & 7);
      const float mm = __shfl(mean, src), rr = __shfl(rstd, src);
      float o[4];
#pragma unroll
      for (int t = 0; t < 4; ++t) o[t] = gelu(rr * (acc[tt][t] - mm * cs1[t]));
      d[tt][0] = pk(o[0], o[1]); d[tt][1] = pk(o[2], o[3]);
      const int tok = 16 * tt + c16, sg = 8 * ((tok >> 1) & 3) + 2 * (tok >> 3) + (tok & 1);
      resh[tt] = *(LAS const v2u*)(L + L_X + (sg >> 4) * 16384 + (16 * g + 2 * w + (qq >> 1)) * 256 + (sg & 15) * 16 + (qq & 1) * 8);
      resx[tt] = *(LAS const v2u*)(L + L_X1 + (sg >> 4) * 4096 + (2 * w + (qq >> 1)) * 256 + (sg & 15) * 16 + (qq & 1) * 8);
    }
    swap16u(d[0][0], d[1][0]); swap16u(d[0][1], d[1][1]);
    const int sg = 16 * (qq & 1) + c16, tok = 32 * T + 8 * ((sg & 7) >> 1) + (sg & 1) + 2 * (sg >> 3);
    st128s(v4u{d[0][0], d[0][1], d[1][0], d[1][1]}, ws, OFF_HH + (tok >> 4) * 16384 + (16 * g + 2 * w + (qq >> 1)) * 256 + (tok & 15) * 16);
  }
  DRAIN();
  BAR();
  if (tid == 0) st32s(tag, ws, OFF_F2 + me * 128);
  STAMP(9);
  {
#pragma unroll
    for (int i = 0; i < 7; ++i) {
      const int n = i * 64 + lane, pos = n / 14, pc = n % 14;
      const int k = 8 * (2 * (pos >> 4) + ((pos >> 2) & 1)) + 4 * ((pos >> 3) & 1) + (pos & 3);
      const char* src = (const char*)p.w3 + (64 * w + k) * 3584 + m * 224 + pc * 16;
      glds16(src, (unsigned)(size_t)(L + L_W3) + 2 * w * 7168 + i * 1024);
      glds16(src + 32 * 3584, (unsigned)(size_t)(L + L_W3) + (2 * w + 1) * 7168 + i * 1024);
    }
  }
  if (w == 0) {
    const int j = lane & 7, dT = j < 4 ? 0 : ((j & 1) ? 1 : -1), Tn = min(max(T + dT, 0), 63);
    poll1(ws, OFF_F2 + (Tn * 4 + (j & 3)) * 128, tag);
  }
  BAR();
  STAMP(10);
  {
    const int gk = w >> 1, shift = gk == 0 ? -1 : gk == 1 ? 1 : gk == 2 ? -2 : 2;
    v4u xv[2][2];
#pragma unroll
    for (int tt = 0; tt < 2; ++tt) {
      const int tok = 32 * T + 16 * tt + c16 + shift;
      const unsigned o = (tok >= 0 && tok < 2048) ? OFF_HH + (tok >> 4) * 16384 + 2 * w * 1024 + qq * 256 + (tok & 15) * 16 : OOB;
      xv[tt][0] = ld128(ws, o, 1); xv[tt][1] = ld128(ws, o + 1024, 1);
    }
    const v2u gbv = ld64s(ws, OFF_MOD + 5120 + (128 * g + 16 * w + 4 * qq) * 2);
#pragma unroll
    for (int tt = 0; tt < 2; ++tt)
#pragma unroll
      for (int i = 0; i < 2; ++i) *(LAS v4u*)(L + L_X + tt * 16384 + (2 * w + i) * 1024 + lane * 16) = xv[tt][i];
    BAR();
    f32x4 acc[2] = {{0.f, 0.f, 0.f, 0.f}, {0.f, 0.f, 0.f, 0.f}};
#pragma unroll
    for (int i = 0; i < 16; ++i)
#pragma unroll
      for (int tt = 0; tt < 2; ++tt) acc[tt] = mfma(wB[i], *(LAS const v4u*)(L + L_X + tt * 16384 + i * 1024 + lane * 16), acc[tt]);
    unsigned d[2][2];
    f32x4 r[2];
#pragma unroll
    for (int tt = 0; tt < 2; ++tt) {
      d[tt][0] = pk(lo(resh[tt].x) + acc[tt][0], hi(resh[tt].x) + acc[tt][1]);
      d[tt][1] = pk(lo(resh[tt].y) + acc[tt][2], hi(resh[tt].y) + acc[tt][3]);
      r[tt] = f32x4{lo(resx[tt].x) + lo(gbv.x) * lo(d[tt][0]), hi(resx[tt].x) + hi(gbv.x) * hi(d[tt][0]),
                    lo(resx[tt].y) + lo(gbv.y) * lo(d[tt][1]), hi(resx[tt].y) + hi(gbv.y) * hi(d[tt][1])};
    }
    swap16u(d[0][0], d[1][0]); swap16u(d[0][1], d[1][1]);
    st128s(v4u{d[0][0], d[0][1], d[1][0], d[1][1]}, ws, OFF_X1P + (2 * T + (qq & 1)) * 16384 + (16 * g + 2 * w + (qq >> 1)) * 256 + c16 * 16);
    DRAIN();
    BAR();
    if (tid == 0) st32s(tag, ws, OFF_F3 + me * 128);
    STAMP(11);
#pragma unroll
    for (int tt = 0; tt < 2; ++tt) st128s(__builtin_bit_cast(v4u, r[tt]), ws, OFF_R + ((2 * T + tt) * 32 + 8 * g + w) * 1024 + lane * 16);
  }
  const int th = m >> 3, cj = m & 7;
  v4u w4[4][7];
#pragma unroll
  for (int n = 0; n < 2; ++n)
#pragma unroll
    for (int i = 0; i < 7; ++i) w4[n][i] = ld128(ws, OFF_W4P + ((4 * cj + n) * 56 + 7 * w + i) * 1024 + lane * 16, 1);
  SCHED();
  LAS char* w3a = L + L_W3 + (16 * (qq >> 1) + 4 * (qq & 1) + (c16 >> 2)) * 224 + (lane & 3) * 8;
  if (w < 7) {
    f32x4 cs = {0.f, 0.f, 0.f, 0.f};
#pragma unroll
    for (int i = 0; i < 16; ++i) {
      const v2u t0 = trread(w3a + i * 7168 + w * 32), t1 = trread(w3a + i * 7168 + 1792 + w * 32);
      cs = mfma(v4u{t0.x, t0.y, t1.x, t1.y}, ones, cs);
    }
    if (c16 == 0) *(LAS f32x4*)(L + L_CS3 + w * 64 + qq * 16) = cs;
  }
  if (w == 7) poll1(ws, OFF_F3 + (16 * squad + c16) * 128, tag);
  BAR();
  STAMP(12);
  {
    v4u xv[16];
#pragma unroll
    for (int i = 0; i < 16; ++i) xv[i] = ld128(ws, OFF_X1P + (8 * squad + w) * 16384 + i * 1024 + lane * 16, 1);
#pragma unroll
    for (int n = 2; n < 4; ++n)
#pragma unroll
      for (int i = 0; i < 7; ++i) w4[n][i] = ld128(ws, OFF_W4P + ((4 * cj + n) * 56 + 7 * w + i) * 1024 + lane * 16, 1);
    SCHED();
    f32x4 acc[7], aS = {0.f, 0.f, 0.f, 0.f}, aQ = {0.f, 0.f, 0.f, 0.f};
#pragma unroll
    for (int n = 0; n < 7; ++n) acc[n] = f32x4{0.f, 0.f, 0.f, 0.f};
#pragma unroll
    for (int i = 0; i < 16; ++i) {
      aS = mfma(ones, xv[i], aS);
      aQ = mfma(xv[i], xv[i], aQ);
#pragma unroll
      for (int n = 0; n < 7; ++n) {
        const v2u t0 = trread(w3a + i * 7168 + n * 32), t1 = trread(w3a + i * 7168 + 1792 + n * 32);
        acc[n] = mfma(v4u{t0.x, t0.y, t1.x, t1.y}, xv[i], acc[n]);
      }
    }
    STAMP(13);
    const int r3 = c16 & 3;
    const float dq = r3 == 0 ? aQ[0] : r3 == 1 ? aQ[1] : r3 == 2 ? aQ[2] : aQ[3];
    float mean, rstd;
    ln_finish(aS[0], __shfl(dq, (c16 >> 2) * 16 + c16), mean, rstd);
    unsigned d[7][2];
#pragma unroll
    for (int n = 0; n < 7; ++n) {
      const f32x4 cs = *(LAS const f32x4*)(L + L_CS3 + n * 64 + qq * 16);
      float o[4];
#pragma unroll
      for (int t = 0; t < 4; ++t) o[t] = gelu(rstd * (acc[n][t] - mean * cs[t]));
      d[n][0] = pk(o[0], o[1]); d[n][1] = pk(o[2], o[3]);
    }
    const unsigned go = OFF_G + (8 * squad + w) * 57344 + m * 3584;
#pragma unroll
    for (int j = 0; j < 3; ++j) st128s(v4u{d[2 * j][0], d[2 * j][1], d[2 * j + 1][0], d[2 * j + 1][1]}, ws, go + j * 1024 + lane * 16);
    unsigned x0 = d[6][0], y0 = d[6][0], x1 = d[6][1], y1 = d[6][1];
    swap16u(x0, y0); swap16u(x1, y1);
    st128s(v4u{x0, x1, y0, y1}, ws, (qq & 1) ? OOB : go + 3072 + (qq >> 1) * 256 + c16 * 16);
  }
  DRAIN();
  BAR();
  if (tid == 0) st32s(tag, ws, OFF_F4 + me * 128);
  STAMP(14);
  if (w == 0) poll1(ws, OFF_F4 + (16 * squad + c16) * 128, tag);
  BAR();
  STAMP(15);
  {
    v4u xv[4][7];
#pragma unroll
    for (int tt = 0; tt < 4; ++tt)
#pragma unroll
      for (int i = 0; i < 7; ++i) xv[tt][i] = ld128(ws, OFF_G + (8 * squad + 4 * th + tt) * 57344 + (7 * w + i) * 1024 + lane * 16, 1);
    const int ett = w >> 1, en = 2 * (w & 1);
    const unsigned ro = OFF_R + ((8 * squad + 4 * th + ett) * 32 + 4 * cj + en) * 1024 + lane * 16;
    v4u r0, r1; v2u g0, g1;
    SCHED();
#pragma unroll
    for (int tt = 0; tt < 4; ++tt) {
      f32x4 acc[4];
#pragma unroll
      for (int n = 0; n < 4; ++n) acc[n] = f32x4{0.f, 0.f, 0.f, 0.f};
#pragma unroll
      for (int i = 0; i < 7; ++i)
#pragma unroll
        for (int n = 0; n < 4; ++n) acc[n] = mfma(w4[n][i], xv[tt][i], acc[n]);
#pragma unroll
      for (int n = 0; n < 4; ++n) *(LAS f32x4*)(L + ((w * 4 + tt) * 4 + n) * 1024 + lane * 16) = acc[n];
      SCHED();
      if (tt == 0) {
        r0 = ld128(ws, ro, 1); r1 = ld128(ws, ro + 1024, 1);
        g0 = ld64s(ws, OFF_MOD + 5120 + (64 * cj + 16 * en + 4 * qq) * 2); g1 = ld64s(ws, OFF_MOD + 5120 + (64 * cj + 16 * en + 16 + 4 * qq) * 2);
        SCHED();
      }
    }
    BAR();
    STAMP(16);
    f32x4 s0 = {0.f, 0.f, 0.f, 0.f}, s1 = {0.f, 0.f, 0.f, 0.f};
#pragma unroll
    for (int v = 0; v < 8; ++v) {
      s0 += *(LAS const f32x4*)(L + ((v * 4 + ett) * 4 + en) * 1024 + lane * 16);
      s1 += *(LAS const f32x4*)(L + ((v * 4 + ett) * 4 + en + 1) * 1024 + lane * 16);
    }
    const f32x4 q0 = __builtin_bit_cast(f32x4, r0), q1 = __builtin_bit_cast(f32x4, r1);
    u16* op = p.out + (128 * squad + 64 * th + 16 * ett + c16) * 512 + 64 * cj + 16 * en + 4 * qq;
    *(GAS v2u*)(op) = v2u{pk(q0[0] + lo(g0.x) * s0[0], q0[1] + hi(g0.x) * s0[1]), pk(q0[2] + lo(g0.y) * s0[2], q0[3] + hi(g0.y) * s0[3])};
    *(GAS v2u*)(op + 16) = v2u{pk(q1[0] + lo(g1.x) * s1[0], q1[1] + hi(g1.x) * s1[1]), pk(q1[2] + lo(g1.y) * s1[2], q1[3] + hi(g1.y) * s1[3])};
  }
  if (b == 0 && tid == 0) st32s(tag, ws, OFF_EPOCH);
  STAMP(17);
  if (w == 0 && b == 100) { const unsigned long long t = __builtin_amdgcn_s_memrealtime(); while (__builtin_amdgcn_s_memrealtime() - t < (dg1 - dg0) * 4) __builtin_amdgcn_s_sleep(1); }
}
}

extern "C" void kernel_launch(void* const* d_in, const int* in_sizes, int n_in, void* d_out, int out_size, void* d_ws,
                              size_t ws_size, hipStream_t stream) {
  mk::Params p;
  p.q = (const mk::u16*)d_in[0]; p.k = (const mk::u16*)d_in[1]; p.v = (const mk::u16*)d_in[2];
  p.sk = (const mk::u16*)d_in[3]; p.ssmk = (const mk::u16*)d_in[4]; p.ssmD = (const mk::u16*)d_in[5];
  p.w1 = (const mk::u16*)d_in[7]; p.w2 = (const mk::u16*)d_in[8]; p.w3 = (const mk::u16*)d_in[9]; p.w4 = (const mk::u16*)d_in[10];
  p.c = (const mk::u16*)d_in[11]; p.wmod = (const mk::u16*)d_in[12];
  p.out = (mk::u16*)d_out; p.ws = (char*)d_ws;
  mk::mega<<<256, 512, 0, stream>>>(p);
}
```

```cpp
#define DIAG 0
#include <hip/hip_runtime.h>
#include <stdint.h>

namespace mk {
typedef unsigned short u16;
typedef unsigned v4u __attribute__((ext_vector_type(4)));
typedef unsigned v2u __attribute__((ext_vector_type(2)));
typedef float f32x4 __attribute__((ext_vector_type(4)));
typedef float f32x2 __attribute__((ext_vector_type(2)));
typedef __bf16 bf16x8 __attribute__((ext_vector_type(8)));
typedef __bf16 bf16x2 __attribute__((ext_vector_type(2)));
typedef short s16x4 __attribute__((ext_vector_type(4)));
typedef __amdgpu_buffer_rsrc_t rsrc_t;
#define LAS __attribute__((address_space(3)))
#define GAS __attribute__((address_space(1)))
#define DEVI __device__ __forceinline__

constexpr int SC1 = 16;
constexpr int OFF_EPOCH = 0;
constexpr int OFF_F0 = 4096;
constexpr int OFF_FR = OFF_F0 + 1024 * 128;
constexpr int OFF_F1 = OFF_FR + 8 * 8192;
constexpr int OFF_F2 = OFF_F1 + 8 * 8192;
constexpr int OFF_F3 = OFF_F2 + 256 * 128;
constexpr int OFF_F4 = OFF_F3 + 256 * 128;
constexpr int OFF_FW = OFF_F4 + 256 * 128;
constexpr int OFF_MOD = OFF_FW + 128 * 128;
constexpr int OFF_ST = 512 << 10;
constexpr int OFF_UR = 1 << 20;
constexpr int OFF_Z = OFF_UR + (2 << 20);
constexpr int OFF_W1P = OFF_Z + (2 << 20);
constexpr int OFF_W2P = OFF_W1P + (512 << 10);
constexpr int OFF_W4P = OFF_W2P + (512 << 10);
constexpr int OFF_HH = OFF_W4P + (2 << 20);
constexpr int OFF_X1P = OFF_HH + (2 << 20);
constexpr int OFF_R = OFF_X1P + (2 << 20);
constexpr int OFF_G = OFF_R + (4 << 20);
constexpr int WS_USED = OFF_G + 128 * 57344;
static_assert(OFF_MOD + 49152 <= OFF_ST, "ws");
constexpr unsigned OOB = 0x80000000u;
constexpr int L_W3 = 0;
constexpr int L_KC = 112256;
constexpr int L_UC = L_KC + 8 * 4160;
constexpr int L_X = 114688;
constexpr int L_X1 = L_X + 32768;
constexpr int L_PART = L_X1 + 8192;
constexpr int L_GB = L_PART;
constexpr int L_MV = 0;
constexpr int L_RED = 78848;
constexpr int L_KT = 0;
constexpr int L_VT = L_KT + 9 * 1040;
constexpr int L_STG = 17920;
constexpr int L_CNT = L_STG + 9216;
constexpr int L_S1 = L_CNT + 64;
constexpr int L_Q = L_S1 + 256;
constexpr int L_WM = L_Q + 32 * 1088;
constexpr int L_SC = L_KT;
static_assert(L_VT + 8 * 1040 <= L_STG && L_WM + 16384 <= L_RED, "lds");
static_assert(L_UC + 4 * 4576 == 163840, "lds");
static_assert(L_PART + 3 * 2048 <= 163840, "lds");

#define STAMP(i) do { if ((i) == 0) dg0 = __builtin_amdgcn_s_memrealtime(); if ((i) == DIAG) dg1 = __builtin_amdgcn_s_memrealtime(); } while (0)

struct Params {
  const u16 *q, *k, *v, *sk, *ssmk, *ssmD, *w1, *w2, *w3, *w4, *c, *wmod;
  u16* out;
  char* ws;
};

DEVI float lo(unsigned u) { return __builtin_bit_cast(float, u << 16); }
DEVI float hi(unsigned u) { return __builtin_bit_cast(float, u & 0xffff0000u); }
DEVI unsigned pk(float a, float b) { f32x2 f = {a, b}; return __builtin_bit_cast(unsigned, __builtin_convertvector(f, bf16x2)); }
DEVI float rb(float a) { return lo(pk(a, a)); }
DEVI f32x4 mfma(v4u a, v4u b, f32x4 c) {
  return __builtin_amdgcn_mfma_f32_16x16x32_bf16(__builtin_bit_cast(bf16x8, a), __builtin_bit_cast(bf16x8, b), c, 0, 0, 0);
}
DEVI rsrc_t mkrs(const void* p, int bytes) { return __builtin_amdgcn_make_buffer_rsrc((void*)p, 0, bytes, 0x00020000); }
#define NTAUX 2
DEVI v4u ld128(rsrc_t r, unsigned off, int aux) { return aux ? __builtin_amdgcn_raw_buffer_load_b128(r, off, 0, SC1) : __builtin_amdgcn_raw_buffer_load_b128(r, off, 0, NTAUX); }
DEVI v4u ld128so(rsrc_t r, unsigned off, unsigned so) { return __builtin_amdgcn_raw_buffer_load_b128(r, off, so, SC1); }
DEVI v2u ld64s(rsrc_t r, unsigned off) { return __builtin_amdgcn_raw_buffer_load_b64(r, off, 0, SC1); }
DEVI unsigned ld32(rsrc_t r, unsigned off, int aux) { return aux ? __builtin_amdgcn_raw_buffer_load_b32(r, off, 0, SC1) : __builtin_amdgcn_raw_buffer_load_b32(r, off, 0, NTAUX); }
DEVI void st128s(v4u v, rsrc_t r, unsigned off) { __builtin_amdgcn_raw_buffer_store_b128(v, r, off, 0, SC1); }
DEVI void st32s(unsigned v, rsrc_t r, unsigned off) { __builtin_amdgcn_raw_buffer_store_b32(v, r, off, 0, SC1); }
#define MEMFENCE() asm volatile("" ::: "memory")
#define DRAIN() asm volatile("s_waitcnt vmcnt(0)" ::: "memory")
#define BAR() asm volatile("s_waitcnt lgkmcnt(0)\n\ts_barrier" ::: "memory")
#define SCHED() __builtin_amdgcn_sched_barrier(0)
constexpr unsigned kSpinCap = 1u << 21;

DEVI void poll1(rsrc_t r, unsigned off, unsigned tag) {
  for (unsigned s = 0; s < kSpinCap; ++s) {
    MEMFENCE();
    const unsigned f = ld32(r, off, 1);
    if (__all(f == tag)) break;
  }
  MEMFENCE();
}
DEVI void poll4(rsrc_t r, unsigned off, unsigned tag) {
  for (unsigned s = 0; s < kSpinCap; ++s) {
    MEMFENCE();
    const v4u f = ld128(r, off, 1);
    if (__all((f.x == tag) & (f.y == tag) & (f.z == tag) & (f.w == tag))) break;
  }
  MEMFENCE();
}
DEVI float fbits(unsigned u) { return __builtin_bit_cast(float, u); }
DEVI unsigned ubits(float f) { return __builtin_bit_cast(unsigned, f); }
DEVI float ror8(float x) { return fbits(__builtin_amdgcn_update_dpp(0u, ubits(x), 0x128, 0xf, 0xf, false)); }
DEVI void swap32(float& a, float& b) { auto r = __builtin_amdgcn_permlane32_swap(ubits(a), ubits(b), false, false); a = fbits(r[0]); b = fbits(r[1]); }
DEVI void swap16(float& a, float& b) { auto r = __builtin_amdgcn_permlane16_swap(ubits(a), ubits(b), false, false); a = fbits(r[0]); b = fbits(r[1]); }
DEVI void swap16u(unsigned& a, unsigned& b) { auto r = __builtin_amdgcn_permlane16_swap(a, b, false, false); a = r[0]; b = r[1]; }

DEVI float ln_wave_reduce(const float (&s)[4], const float (&qv)[4], int lane) {
  float a0 = s[0], b0 = s[2], a1 = s[1], b1 = s[3], c0 = qv[0], d0 = qv[2], c1 = qv[1], d1 = qv[3];
  swap32(a0, b0); swap32(a1, b1); swap32(c0, d0); swap32(c1, d1);
  float s0 = a0 + b0, s1 = a1 + b1, q0 = c0 + d0, q1 = c1 + d1;
  swap16(s0, s1); swap16(q0, q1);
  const float x = s0 + s1, y = q0 + q1;
  const bool b3 = lane & 8;
  return (b3 ? y : x) + ror8(b3 ? x : y);
}
DEVI void ln_finish(float sum, float sq, float& mean, float& sd) {
  const float m = sum * (1.f / 512.f);
  const float var = fmaxf(sq * (1.f / 512.f) - m * m, 0.f);
  mean = rb(m);
  sd = rb(__builtin_amdgcn_sqrtf(rb(rb(var) + 1e-5f)));
}
DEVI void ln_total(LAS const float* part, int lane, float& mean, float& sd) {
  float t = 0.f;
#pragma unroll
  for (int w = 0; w < 8; ++w) t += part[w * 64];
  const float o = ror8(t);
  const bool b3 = lane & 8;
  ln_finish(b3 ? o : t, b3 ? t : o, mean, sd);
}
DEVI void gather4(float x, float (&o)[4]) {
  float e = x, d = x;
  swap16(e, d);
  float e2 = e, d2 = d;
  swap32(e, e2); swap32(d, d2);
  o[0] = e; o[2] = e2; o[1] = d; o[3] = d2;
}
DEVI float gelu(float x) {
  const float t = x * (2.302208198f + 0.1029432397f * x * x);
  return x * __builtin_amdgcn_rcpf(1.f + __builtin_amdgcn_exp2f(-t));
}
DEVI void glds16(const void* gsrc, unsigned lds_dst) {
  unsigned keep;
  asm volatile("s_mov_b32 %0, m0\n\ts_mov_b32 m0, %2\n\ts_nop 0\n\tglobal_load_lds_dwordx4 %1, off\n\ts_mov_b32 m0, %0" : "=&s"(keep) : "v"(gsrc), "s"(lds_dst) : "memory");
}
DEVI void glds16s(unsigned voff, const void* sbase, unsigned lds_dst) {
  unsigned keep;
  asm volatile("s_mov_b32 %0, m0\n\ts_mov_b32 m0, %3\n\ts_nop 0\n\tglobal_load_lds_dwordx4 %1, %2\n\ts_mov_b32 m0, %0" : "=&s"(keep) : "v"(voff), "s"(sbase), "s"(lds_dst) : "memory");
}
DEVI void glds16nt(unsigned voff, const void* sbase, unsigned lds_dst) {
  unsigned keep;
  asm volatile("s_mov_b32 %0, m0\n\ts_mov_b32 m0, %3\n\ts_nop 0\n\tglobal_load_lds_dwordx4 %1, %2 nt\n\ts_mov_b32 m0, %0" : "=&s"(keep) : "v"(voff), "s"(sbase), "s"(lds_dst) : "memory");
}
#define CODEB 30
constexpr int CODE_BYTES = CODEB * 1024;
DEVI v2u trread(LAS char* a) { return __builtin_bit_cast(v2u, __builtin_amdgcn_ds_read_tr16_b64_v4i16((LAS s16x4*)a)); }

DEVI f32x2 up(unsigned u) { return f32x2{lo(u), hi(u)}; }
DEVI unsigned pk2(f32x2 f) { return __builtin_bit_cast(unsigned, __builtin_convertvector(f, bf16x2)); }
DEVI f32x2 fma2(f32x2 a, f32x2 b, f32x2 c) { return __builtin_elementwise_fma(a, b, c); }
DEVI f32x2 sp(float a) { return f32x2{a, a}; }
DEVI unsigned gelu2(f32x2 x) {
  const f32x2 t = x * fma2(x * x, sp(-0.1029432397f), sp(-2.302208198f));
  const f32x2 e = f32x2{__builtin_amdgcn_exp2f(t.x), __builtin_amdgcn_exp2f(t.y)} + sp(1.f);
  return pk2(x * f32x2{__builtin_amdgcn_rcpf(e.x), __builtin_amdgcn_rcpf(e.y)});
}
DEVI float dot2(unsigned a, unsigned b, float c) {
  return __builtin_amdgcn_fdot2_f32_bf16(__builtin_bit_cast(bf16x2, a), __builtin_bit_cast(bf16x2, b), c, false);
}
DEVI float gelu_s(float a, float k1, float k2, float sd) {
  const float e = __builtin_amdgcn_exp2f(a * __builtin_fmaf(a * a, k2, k1));
  float o = a * __builtin_amdgcn_rcpf(__builtin_fmaf(e, sd, sd));
  asm("" : "+v"(o));
  return o;
}
DEVI unsigned gelu_p(f32x2 a, float k1, float k2, float sd) {
  const f32x2 t = a * fma2(a * a, sp(k2), sp(k1));
  const f32x2 d = fma2(f32x2{__builtin_amdgcn_exp2f(t.x), __builtin_amdgcn_exp2f(t.y)}, sp(sd), sp(sd));
  return pk2(a * f32x2{__builtin_amdgcn_rcpf(d.x), __builtin_amdgcn_rcpf(d.y)});
}
template <int CTRL> DEVI float dppf(float x) { return fbits(__builtin_amdgcn_update_dpp(0u, ubits(x), CTRL, 0xf, 0xf, false)); }
DEVI void lds_wait(LAS char* L, int word, unsigned want) {
#pragma nounroll
  for (unsigned s = 0; s < (1u << 26); ++s) {
    if (*(volatile LAS unsigned*)(L + L_CNT + 4 * word) >= want) break;
    __builtin_amdgcn_s_sleep(1);
  }
  MEMFENCE();
}
DEVI unsigned lds_inc(LAS char* L, int word, int lane) {
  unsigned old = 0;
  asm volatile("s_waitcnt lgkmcnt(0)" ::: "memory");
  if (lane == 0) old = __hip_atomic_fetch_add((LAS unsigned*)(L + L_CNT + 4 * word), 1u, __ATOMIC_RELAXED, __HIP_MEMORY_SCOPE_WORKGROUP);
  return __builtin_amdgcn_readfirstlane(old);
}
template <typename T> DEVI T opaque(T x) { asm volatile("" : "+v"(x)); return x; }
DEVI LAS char* ldsp(unsigned a) { return (LAS char*)(size_t)a; }

__global__ void __launch_bounds__(512, 2) mega(Params p) {
  __shared__ __attribute__((aligned(16))) char smem[163840];
  LAS char* L = (LAS char*)smem;
  const unsigned LB = (unsigned)(size_t)L;
  unsigned long long dg0 = 0, dg1 = 0;
  STAMP(0);
  unsigned long long pc;
  asm volatile("s_getpc_b64 %0" : "=s"(pc));
  const int tid = threadIdx.x, lane = tid & 63, w = __builtin_amdgcn_readfirstlane(tid >> 6);
  const int b = blockIdx.x, team = b & 7, rank = b >> 3, me = team * 32 + rank;
  const int c16 = lane & 15, qq = lane >> 4;
#define MAP2D 0
  const int squad = MAP2D ? 4 * (team >> 1) + (rank >> 3) : me >> 4, m = MAP2D ? 8 * (team & 1) + (rank & 7) : me & 15;
  const int T = MAP2D ? 4 * squad + ((rank & 7) >> 1) : me >> 2, g = MAP2D ? 2 * (team & 1) + (rank & 1) : me & 3, me2 = 4 * T + g;
  const int g8 = lane >> 3, tg = lane & 7;
  const rsrc_t ws = mkrs(p.ws, WS_USED);
#define BGOFF 0
  const rsrc_t wsW12 = mkrs(p.ws, (BGOFF & 1) ? 0 : WS_USED), wsW4 = mkrs(p.ws, (BGOFF & 2) ? 0 : WS_USED), wsRT = mkrs(p.ws, (BGOFF & 4) ? 0 : WS_USED);

  const unsigned wo = ((8 * g + w) * 16) * 1024 + lane * 16;
  const unsigned tile_o = (64 * rank + g8) * 1024 + team * 128 + tg * 16;
  v4u wA[16], wB[16];
  unsigned tag;
#define RETILE(DST, STR) do { \
    LAS char* stg = L + L_STG; \
    _Pragma("unroll") for (int j = 0; j < 8; ++j) *(LAS v4u*)(stg + (qq * 8 + j) * 288 + c16 * 16) = raw[j]; \
    asm volatile("s_waitcnt lgkmcnt(0)" ::: "memory"); \
    _Pragma("unroll") for (int i = 0; i < 8; ++i) { \
      LAS char* a = stg + (qq * 8 + (c16 >> 2)) * 288 + i * 32 + (lane & 3) * 8; \
      const v2u t0 = trread(a), t1 = trread(a + 4 * 288); \
      __builtin_amdgcn_raw_buffer_store_b128(v4u{t0.x, t0.y, t1.x, t1.y}, wsRT, lane * 16, (DST) + i * (STR), SC1); \
    } } while (0)
  if (w < 4) {
    const unsigned vo = tile_o + w * 16384;
    glds16nt(vo, p.k, LB + L_KT + (2 * w + 1) * 1040);
    glds16nt(vo, p.v, LB + L_VT + (2 * w) * 1040);
    glds16nt(vo + 8192, p.k, LB + L_KT + (2 * w + 2) * 1040);
    glds16nt(vo + 8192, p.v, LB + L_VT + (2 * w + 1) * 1040);
    if (w == 0) glds16nt(rank ? vo - 8192 : vo, p.k, LB + L_KT);
  }
  const v4u sk = *(const GAS v4u*)(p.sk + (64 * team + 2 * (8 * (w & 3) + g8)) * 4);
  const unsigned epoch = ld32(ws, OFF_EPOCH, 1);
  if (tid >= 504) *(LAS unsigned*)(L + L_CNT + (tid & 7) * 4) = 0u;
  BAR();
#define NWB 8
  const v4u ones = {0x3F803F80u, 0x3F803F80u, 0x3F803F80u, 0x3F803F80u};
  v4u kp[4], raw5[8];
  if (w >= 4) {
    const rsrc_t rssm = mkrs(p.ssmk, (BGOFF & 64) ? 0 : 512 * 2048 * 2);
    const int a = tid & 255;
    const unsigned o = (unsigned)(2 * me * 4096 + a * 16);
    kp[1] = ld128(rssm, o, 0); kp[3] = ld128(rssm, o + 4096, 0);
    kp[0] = ld128(rssm, a ? o - 16 : OOB, 0); kp[2] = ld128(rssm, a ? o + 4080 : OOB, 0);
    if (w == 5) {
      const int u = b >> 1;
      const rsrc_t rw = mkrs(u < 64 ? p.w1 : p.w2, ((BGOFF & 4) || (b & 1)) ? 0 : 512 * 1024);
#pragma unroll
      for (int j = 0; j < 8; ++j) raw5[j] = ld128(rw, (unsigned)((32 * (u & 15) + 8 * qq) * 1024 + 256 * ((u >> 4) & 3) + 16 * c16) + j * 1024, 0);
    }
    if (w == 4) raw5[0] = *(const GAS v4u*)(p.c + 8 * lane);
    if (w == 7) {
      const int j = lane & 15;
      unsigned fo = OFF_F0 + ((team * 4 + j) * 32 + rank) * 128;
      fo = j >= 4 ? OFF_FR + (j & 1) * 8 * 8192 + ((b & 3) * 2 + ((j >> 1) & 1)) * 8192 + (b >> 2) * 128 : fo;
      fo = j >= 8 ? OFF_F2 + (j - 8) * 32768 + me2 * 128 : fo;
      fo = j >= 11 ? OFF_FW + (b >> 1) * 128 : fo;
      const unsigned x = ld32(ws, fo, 1);
      const unsigned y = *(const GAS unsigned*)((pc & ~255ull) + (size_t)min(rank * 2048 + lane * 32, CODE_BYTES));
      asm volatile("" ::"v"(x), "v"(y));
    }
  }
  SCHED();
  if (__builtin_expect(w < 4, 1)) {
    LAS const char* kt = ldsp(opaque(LB + L_KT + tg * 1040 + (8 * w + g8) * 4));
    const unsigned uo = opaque(OFF_UR + (64 * team + 2 * (8 * w + g8)) * 4096 + (2040 - 64 * rank - 8 * tg) * 2);
    __builtin_amdgcn_s_setprio(3);
    DRAIN();
    if (lane == 0) __hip_atomic_fetch_add((LAS unsigned*)(L + L_CNT + 20), 1u, __ATOMIC_RELAXED, __HIP_MEMORY_SCOPE_WORKGROUP);
    for (unsigned s = 0; s < (1u << 26); ++s)
      if (*(volatile LAS unsigned*)(L + L_CNT + 20) >= 4) break;
    MEMFENCE();
    STAMP(24);
    {
      unsigned kk[11], vv[8];
#pragma unroll
      for (int i = 0; i < 3; ++i) kk[i] = *(LAS const unsigned*)(kt + 640 + 128 * i);
#pragma unroll
      for (int i = 0; i < 8; ++i) { kk[i + 3] = *(LAS const unsigned*)(kt + 1040 + 128 * i); vv[i] = *(LAS const unsigned*)(kt + L_VT + 128 * i); }
      if (rank == 0 && tg == 0) { kk[0] = 0u; kk[1] = 0u; kk[2] = 0u; }
      const f32x2 a0 = {lo(sk.x), lo(sk.z)}, a1 = {hi(sk.x), hi(sk.z)}, a2 = {lo(sk.y), lo(sk.w)}, a3 = {hi(sk.y), hi(sk.w)};
      f32x2 kf[11], x[8];
#pragma unroll
      for (int i = 0; i < 11; ++i) kf[i] = up(kk[i]);
#pragma unroll
      for (int t = 0; t < 8; ++t) x[t] = fma2(kf[t + 3], a0, fma2(kf[t + 2], a1, fma2(kf[t + 1], a2, kf[t] * a3))) * up(vv[t]);
      st128s(v4u{pk(x[7].x, x[6].x), pk(x[5].x, x[4].x), pk(x[3].x, x[2].x), pk(x[1].x, x[0].x)}, ws, uo);
      st128s(v4u{pk(x[7].y, x[6].y), pk(x[5].y, x[4].y), pk(x[3].y, x[2].y), pk(x[1].y, x[0].y)}, ws, uo + 4096);
    }
    MEMFENCE();
    if (lane == 0) __hip_atomic_fetch_add((LAS unsigned*)(L + L_CNT + 24), 1u, __ATOMIC_RELAXED, __HIP_MEMORY_SCOPE_WORKGROUP);
    SCHED();
    STAMP(22);
    tag = __builtin_amdgcn_readfirstlane(epoch * 0x9E3779B1u + 0x7F4A7C15u);
    const unsigned o = opaque(OFF_UR + (2 * me + (w >> 1)) * 4096 + lane * 16);
    LAS char* uc = ldsp(opaque(LB + L_UC + (w >> 1) * 2 * 4576 + lane * 16));
    LAS const char* pa = ldsp(opaque(LB + L_KC + ((w >> 1) * 4 + (c16 & 3)) * 4160 + (qq + (c16 >> 2)) * 16 + (w & 1) * 256));
    LAS const char* pb = ldsp(opaque(LB + L_UC + ((w >> 1) * 2 + (c16 & 1)) * 4576 + (7 - (c16 >> 1)) * 64 + qq * 16 + (w & 1) * 256));
    LAS char* rp = ldsp(opaque(LB + L_RED + w * 8192 + (8 * (c16 >> 1) + ((2 * qq + (c16 & 1)) ^ (c16 & 6))) * 16));
    DRAIN();
    if (lane == 0) st32s(tag, ws, OFF_F0 + ((team * 4 + w) * 32 + rank) * 128);
    STAMP(1);
    lds_wait(L, 0, 4);
    if (w == 0) {
      poll1(ws, OFF_F0 + ((team * 4 + (rank >> 3)) * 32 + (lane & 31)) * 128, tag);
      if (lane == 0) *(volatile LAS unsigned*)(L + L_CNT + 8) = 1u;
    } else {
      lds_wait(L, 2, 1);
    }
    STAMP(2);
    v4u fa[2][8], fb[2][8];
    {
      v4u u1[4]; unsigned nx[4];
#pragma unroll
      for (int j = 0; j < 4; ++j) { u1[j] = ld128(ws, o + j * 1024, 1); nx[j] = ld32(ws, (j == 3 && lane == 63) ? OOB : o + j * 1024 + 16, 1); }
#pragma unroll
      for (int i = 0; i < 8; ++i) fa[0][i] = *(LAS const v4u*)(pa + 512 * i);
      SCHED();
#pragma unroll
      for (int j = 0; j < 4; ++j) {
        *(LAS v4u*)(uc + 4576 + j * 1024) = u1[j];
        *(LAS v4u*)(uc + j * 1024) = v4u{__builtin_amdgcn_alignbit(u1[j].y, u1[j].x, 16), __builtin_amdgcn_alignbit(u1[j].z, u1[j].y, 16),
                                         __builtin_amdgcn_alignbit(u1[j].w, u1[j].z, 16), __builtin_amdgcn_alignbit(nx[j], u1[j].w, 16)};
      }
      if (lane == 0) __hip_atomic_fetch_add((LAS unsigned*)(L + L_CNT + 12), 1u, __ATOMIC_RELAXED, __HIP_MEMORY_SCOPE_WORKGROUP);
      MEMFENCE();
    }
    STAMP(3);
    f32x4 cacc[8];
#pragma unroll
    for (int gi = 0; gi < 8; ++gi) cacc[gi] = f32x4{0.f, 0.f, 0.f, 0.f};
#pragma unroll
    for (int i = 0; i < 8; ++i) fb[0][i] = *(LAS const v4u*)(pb + 512 * (7 - i));
    SCHED();
#pragma unroll
    for (int t = 0; t < 4; ++t) {
      if (t < 3) {
        fa[(t + 1) & 1][0] = *(LAS const v4u*)(pa + 64 * (t + 1));
#pragma unroll
        for (int i = 0; i < 8; ++i) fb[(t + 1) & 1][i] = *(LAS const v4u*)(pb + 64 * (t + 1) + 512 * (7 - i));
#pragma unroll
        for (int i = 1; i < 8; ++i) fa[(t + 1) & 1][i] = *(LAS const v4u*)(pa + 64 * (t + 1) + 512 * i);
      }
#pragma unroll
      for (int i = 0; i < 8; ++i) {
#pragma unroll
        for (int n = 0; n + i < 8; ++n) cacc[i + n] = mfma(fa[t & 1][i], fb[t & 1][n], cacc[i + n]);
        if (t == 3) {
          if (i > 0) *(LAS f32x4*)(rp + (i - 1) * 1024) = cacc[i - 1];
          SCHED();
        }
      }
      if (t < 3) {
        __builtin_amdgcn_sched_group_barrier(0x8, 4, 0);
#pragma unroll
        for (int i = 0; i < 16; ++i) {
          __builtin_amdgcn_sched_group_barrier(0x100, 1, 0);
          __builtin_amdgcn_sched_group_barrier(0x8, 2, 0);
        }
      }
      SCHED();
    }
    STAMP(23);
    *(LAS f32x4*)(rp + 7 * 1024) = cacc[7];
    __builtin_amdgcn_s_setprio(0);
  } else {
    v4u raw[8];
    const bool dow4 = w == 5 && b < 224;
    const int cb4 = b / 56, ks4 = b % 56, P4 = 4 * ks4 + qq, sl4 = P4 / 14, pp4 = P4 % 14;
    const int k04 = 112 * sl4 + (pp4 < 12 ? 32 * (pp4 >> 2) + 4 * (pp4 & 3) : 96 + 8 * (pp4 - 12));
    const unsigned o0 = opaque(k04 * 1024 + 256 * cb4 + 16 * c16), o1 = opaque(o0 + (pp4 < 12 ? 16 : 4) * 1024);
    {
      const v4u z4 = {0u, 0u, 0u, 0u};
      if (w == 4 && (lane & 3) < 3 && lane < 32) *(LAS v4u*)(L + L_KC + (lane >> 2) * 4160 + (lane & 3) * 16) = z4;
      if (w >= 6 && (lane & 31) < 30) *(LAS v4u*)(L + L_UC + ((w & 1) * 2 + (lane >> 5)) * 4576 + (256 + (lane & 31)) * 16) = z4;
      LAS char* kc = L + L_KC + ((tid & 255) + 3) * 16;
#pragma unroll
      for (int ch = 0; ch < 2; ++ch) {
        const v4u k0 = kp[2 * ch], k1 = kp[2 * ch + 1];
        *(LAS v4u*)(kc + (4 * ch + 0) * 4160) = v4u{k0.y, k0.z, k0.w, k1.x};
        *(LAS v4u*)(kc + (4 * ch + 1) * 4160) = v4u{k0.z, k0.w, k1.x, k1.y};
        *(LAS v4u*)(kc + (4 * ch + 2) * 4160) = v4u{k0.w, k1.x, k1.y, k1.z};
        *(LAS v4u*)(kc + (4 * ch + 3) * 4160) = k1;
      }
      lds_inc(L, 0, lane);
    }
    DRAIN();
    tag = __builtin_amdgcn_readfirstlane(epoch * 0x9E3779B1u + 0x7F4A7C15u);
    STAMP(25);
    if (w == 5) {
      if ((b & 1) == 0) {
        const int u = b >> 1;
#pragma unroll
        for (int j = 0; j < 8; ++j) raw[j] = raw5[j];
        RETILE(OFF_W1P + ((u >> 4) * 8 * 16 + (u & 15)) * 1024, 16 * 1024);
        DRAIN();
        if (lane == 0) st32s(tag, ws, OFF_FW + u * 128);
      }
      if (dow4) {
        const rsrc_t rw = mkrs(p.w4, (BGOFF & 4) ? 0 : 1792 * 1024);
        lds_wait(L, 6, 4);
#pragma unroll
        for (int j = 0; j < 8; ++j) raw[j] = ld128(rw, (j < 4 ? o0 : o1) + (j & 3) * 1024, 0);
        lds_wait(L, 3, 4);
        RETILE(OFF_W4P + (cb4 * 8 * 56 + ks4) * 1024, 56 * 1024);
      }
    } else if (w == 4) {
      v4u sv;
#pragma unroll
      for (int j = 0; j < 4; ++j) {
        const f32x2 x = up(raw5[0][j]);
        sv[j] = pk(x.x * __builtin_amdgcn_rcpf(1.f + __builtin_amdgcn_exp2f(-1.442695041f * x.x)),
                   x.y * __builtin_amdgcn_rcpf(1.f + __builtin_amdgcn_exp2f(-1.442695041f * x.y)));
      }
      lds_wait(L, 6, 4);
      STAMP(26);
      if (b < 192) {
        const int kq = b & 3, cb = b >> 2;
        if (!(BGOFF & 8)) {
#pragma unroll
          for (int i = 0; i < 16; ++i)
            glds16nt(g8 * 6144 + tg * 16, (const char*)p.wmod + (128 * kq + 8 * i) * 6144 + cb * 128, LB + L_WM + i * 1024);
        }
        STAMP(27);
        *(LAS v4u*)(L + L_SC + lane * 16) = sv;
        v4u sf[4];
#pragma unroll
        for (int i = 0; i < 4; ++i) sf[i] = *(LAS const v4u*)(L + L_SC + kq * 256 + i * 64 + qq * 16);
        LAS char* a = ldsp(opaque(LB + L_WM + qq * 1024 + (c16 >> 2) * 128 + (lane & 3) * 8));
        DRAIN();
        STAMP(34);
        f32x4 acc[4];
#pragma unroll
        for (int n = 0; n < 4; ++n) {
          acc[n] = f32x4{0.f, 0.f, 0.f, 0.f};
#pragma unroll
          for (int i = 0; i < 4; ++i) {
            const v2u t0 = trread(a + i * 4096 + n * 32), t1 = trread(a + i * 4096 + n * 32 + 512);
            acc[n] = mfma(v4u{t0.x, t0.y, t1.x, t1.y}, sf[i], acc[n]);
          }
        }
        const f32x4 r = c16 == 0 ? acc[0] : c16 == 1 ? acc[1] : c16 == 2 ? acc[2] : acc[3];
        st128s(__builtin_bit_cast(v4u, r), ws, c16 < 4 ? OFF_MOD + kq * 12288 + cb * 256 + c16 * 64 + qq * 16 : OOB);
        DRAIN();
      }
      if (lane < 8) st32s(tag, ws, OFF_FR + lane * 8192 + (b >> 2) * 128 + (b & 3) * 4);
    }
    STAMP(18);
    if (w == 7) {
      lds_wait(L, 2, 1);
      poll1(ws, OFF_FW + ((lane & 16) * 4 + 16 * g + c16) * 128, tag);
      lds_inc(L, 3, lane);
    }
    lds_wait(L, 3, 5);
    STAMP(19);
    if (w >= 6 && !(BGOFF & 16)) {
#pragma unroll
      for (int j = 0; j < 16; ++j)
        glds16nt(lane * 16, (const char*)p.q + (32 * T + 8 * ((j & 7) >> 1) + (j & 1) + 2 * (2 * (w & 1) + (j >> 3))) * 1024, LB + L_Q + (16 * (w & 1) + j) * 1088);
    }
#pragma unroll
    for (int i = 0; i < 16; ++i) wA[i] = ld128(wsW12, OFF_W1P + wo + i * 1024, 1);
#pragma unroll
    for (int i = 0; i < NWB; ++i) wB[i] = ld128(wsW12, OFF_W2P + wo + i * 1024, 1);
    SCHED();
    STAMP(20);
  }
  const unsigned dd = *(const __attribute__((address_space(4))) unsigned*)(p.ssmD + 2 * me);
  LAS const char* rq = ldsp(opaque(LB + L_RED + w * 1024 + (lane ^ (2 * (g8 & 3))) * 16));
  LAS const char* uq = ldsp(opaque(LB + L_UC + 4576 + (2040 - (256 * w + 32 * g8 + 8 * (tg >> 1))) * 2));
  BAR();
  STAMP(4);
  {
    const f32x4 y0 = *(LAS const f32x4*)(rq) + *(LAS const f32x4*)(rq + 8192);
    const f32x4 y1 = *(LAS const f32x4*)(rq + 16384) + *(LAS const f32x4*)(rq + 24576);
    const v4u k0 = *(LAS const v4u*)(uq), k1 = *(LAS const v4u*)(uq + 2 * 4576);
    const unsigned d0 = (tg & 1) ? (dd & 0xffffu) : (dd << 16), d1 = (tg & 1) ? (dd >> 16) : (dd & 0xffff0000u);
    v4u o;
#pragma unroll
    for (int t = 0; t < 4; ++t) o[t] = pk(dot2(k0[3 - t], d0, y0[t]), dot2(k1[3 - t], d1, y1[t]));
    st128s(o, ws, OFF_Z + (8 * w + g8) * 32768 + me * 128 + tg * 16);
  }
  if (w == 4) poll4(ws, OFF_FR + team * 8192 + lane * 128, tag);
  DRAIN();
  BAR();
  if (tid < 8) st32s(tag, ws, OFF_F1 + tid * 8192 + (b >> 2) * 128 + (b & 3) * 4);
  STAMP(5);

  if (w >= 6) {
    LAS const char* a = ldsp(opaque(LB + L_Q + (16 * (w & 1) + c16) * 1088 + qq * 16));
    f32x4 aS = {0.f, 0.f, 0.f, 0.f}, aQ = {0.f, 0.f, 0.f, 0.f};
    v4u xf[16];
#pragma unroll
    for (int i = 0; i < 16; ++i) xf[i] = *(LAS const v4u*)(a + i * 64);
#pragma unroll
    for (int i = 0; i < 16; ++i) { aS = mfma(ones, xf[i], aS); aQ = mfma(xf[i], xf[i], aQ); }
    const int r3 = c16 & 3;
    float mean, sd;
    ln_finish(aS[0], r3 == 0 ? aQ[0] : r3 == 1 ? aQ[1] : r3 == 2 ? aQ[2] : aQ[3], mean, sd);
    const float rs = __builtin_amdgcn_rcpf(sd);
    if (qq == (c16 >> 2)) *(LAS f32x2*)(L + L_S1 + (c16 & 7) * 32 + (2 * (w & 1) + (c16 >> 3)) * 8) = f32x2{-mean * rs, rs};
  } else {
    v4u pv[2][4];
#pragma unroll
    for (int h = 0; h < 2; ++h)
#pragma unroll
      for (int k = 0; k < 4; ++k) pv[h][k] = ld128(ws, OFF_MOD + k * 12288 + w * 2048 + h * 1024 + lane * 16, 1);
    SCHED();
    const float one = (w == 1 || w == 4) ? 1.f : 0.f;
#pragma unroll
    for (int h = 0; h < 2; ++h) {
      f32x4 v = (__builtin_bit_cast(f32x4, pv[h][0]) + __builtin_bit_cast(f32x4, pv[h][1])) + (__builtin_bit_cast(f32x4, pv[h][2]) + __builtin_bit_cast(f32x4, pv[h][3]));
      const f32x2 a = up(pk2(up(pk(v[0], v[1])) + sp(one))), c = up(pk2(up(pk(v[2], v[3])) + sp(one)));
      *(LAS f32x4*)(L + (w == 5 ? L_GB : L_MV + w * 2048) + h * 1024 + lane * 16) = f32x4{a.x, a.y, c.x, c.y};
    }
  }
  unsigned ghl[4][4], ghh[4][4];
  f32x2 x[4][4], cb1[4], sb2[4];
  {
    LAS const char* qp = ldsp(opaque(LB + L_Q + tg * 1088 + (8 * w + g8) * 16));
    v4u qv[4];
#pragma unroll
    for (int t = 0; t < 4; ++t) qv[t] = *(LAS const v4u*)(qp + t * 8704);
#define LDW(j) do { SCHED(); if (w < 4) { _Pragma("unroll") for (int i_ = 0; i_ < 4; ++i_) { \
      if ((j) < 4) wA[4 * (j) + i_] = ld128(wsW12, OFF_W1P + wo + (4 * (j) + i_) * 1024, 1); \
      else if (4 * ((j) - 4) + i_ < NWB) wB[4 * ((j) - 4) + i_] = ld128(wsW12, OFF_W2P + wo + (4 * ((j) - 4) + i_) * 1024, 1); } } SCHED(); } while (0)
    LDW(0);
#pragma unroll
    for (int t = 0; t < 4; ++t)
#pragma unroll
      for (int i = 0; i < 4; ++i) { x[t][i] = up(qv[t][i]); asm volatile("" : "+v"(x[t][i])); }
    LDW(1);
    BAR();
    const f32x4 sA = *(LAS const f32x4*)(L + L_S1 + tg * 32), sB = *(LAS const f32x4*)(L + L_S1 + tg * 32 + 16);
    const float mm[4] = {sA[0], sA[2], sB[0], sB[2]}, rr[4] = {sA[1], sA[3], sB[1], sB[3]};
    LAS const char* mp = ldsp(opaque(LB + L_MV + (64 * w + 8 * g8) * 4));
    f32x4 mv[5][2];
#pragma unroll
    for (int v = 0; v < 5; ++v) { mv[v][0] = *(LAS const f32x4*)(mp + v * 2048); mv[v][1] = *(LAS const f32x4*)(mp + v * 2048 + 16); }
#define MV2(v, i) f32x2{mv[v][(i) >> 1][2 * ((i) & 1)], mv[v][(i) >> 1][2 * ((i) & 1) + 1]}
#pragma unroll
    for (int i = 0; i < 4; ++i) {
      const f32x2 A = MV2(2, i) * MV2(1, i), B = MV2(2, i) * MV2(0, i);
#pragma unroll
      for (int t = 0; t < 4; ++t) {
        const f32x2 u = fma2(fma2(x[t][i], sp(rr[t]), sp(mm[t])), A, B);
        ghl[t][i] = pk(u.x, 0.f); ghh[t][i] = pk(0.f, u.y);
        asm volatile("" : "+v"(ghl[t][i]), "+v"(ghh[t][i]));
      }
      cb1[i] = MV2(4, i); sb2[i] = MV2(3, i);
      LDW(2 + i);
    }
    LDW(6); LDW(7);
  }
  LAS float* part = (LAS float*)ldsp(opaque(LB + L_PART + lane * 4));
  unsigned w3o;
  {
    const int n = min(w, 6) * 64 + lane, pos = (n * 4682) >> 16, pc = n - 14 * pos;
    w3o = opaque((unsigned)(((pos & 19) | ((pos & 4) << 1) | ((pos & 8) >> 1)) * 3584 + m * 224 + pc * 16));
  }
#define W3DMA(ks) do { SCHED(); if (!(BGOFF & 32) && w < 7) glds16s(w3o, (const char*)p.w3 + (ks) * (32 * 3584), LB + L_W3 + (ks) * 7168 + w * 1024); SCHED(); } while (0)
#define BG2(j) do { W3DMA(j); if (NWB + (j) < 16) { wB[(NWB + (j)) & 15] = ld128(wsW12, OFF_W2P + wo + (NWB + (j)) * 1024, 1); SCHED(); } } while (0)
  const unsigned zo = opaque(OFF_Z + T * 32768 + (32 * w + 4 * g8) * 128 + tg * 16);
  LAS char* xw = ldsp(opaque(LB + L_X + (tg >> 2) * 16384 + (8 * w + g8) * 256 + ((tg & 2) * 4 + (tg & 1)) * 16));
  STAMP(6);
  if (w == 7) poll4(ws, OFF_F1 + team * 8192 + lane * 128, tag);
  BAR();
  STAMP(7);
  f32x4 cs1 = {0.f, 0.f, 0.f, 0.f};
  {
    v4u zz[4];
#pragma unroll
    for (int i = 0; i < 4; ++i) zz[i] = ld128(ws, zo + i * 128, 1);
    SCHED();
#pragma unroll
    for (int i = 0; i < 16; ++i) cs1 = mfma(wA[i], ones, cs1);
    SCHED();
    f32x2 s2[4], q2[4];
#pragma unroll
    for (int i = 0; i < 4; ++i) {
#pragma unroll
      for (int t = 0; t < 4; ++t) {
        x[t][i].x = dot2(zz[i][t], ghl[t][i], x[t][i].x);
        x[t][i].y = dot2(zz[i][t], ghh[t][i], x[t][i].y);
      }
      if (i == 3) { BG2(0); }
#pragma unroll
      for (int t = 0; t < 4; ++t) {
        s2[t] = i ? s2[t] + x[t][i] : x[t][i];
        q2[t] = i ? fma2(x[t][i], x[t][i], q2[t]) : x[t][i] * x[t][i];
      }
      SCHED();
    }
    float s[4], sq[4];
#pragma unroll
    for (int t = 0; t < 4; ++t) { s[t] = s2[t].x + s2[t].y; sq[t] = q2[t].x + q2[t].y; }
    part[512 + w * 64] = ln_wave_reduce(s, sq, lane);
    BG2(1);
    BAR();
    STAMP(35);
    BG2(2);
    float mean, sd, mm[4], rr[4];
    ln_total(part + 512, lane, mean, sd);
    const float rstd = __builtin_amdgcn_rcpf(sd);
    gather4(-mean * rstd, mm); gather4(rstd, rr);
    BG2(3);
    const int sw = (tg >> 1) * 32;
#pragma unroll
    for (int t = 0; t < 4; ++t) {
      v4u o; s[t] = 0.f; sq[t] = 0.f;
#pragma unroll
      for (int i = 0; i < 4; ++i) {
        o[i] = pk2(fma2(fma2(x[t][i], sp(rr[t]), sp(mm[t])), cb1[i], sb2[i]));
        s[t] = dot2(o[i], 0x3F803F80u, s[t]); sq[t] = dot2(o[i], o[i], sq[t]);
      }
      *(LAS v4u*)(xw + ((t * 32) ^ sw)) = o;
      BG2(4 + t);
    }
    part[1024 + w * 64] = ln_wave_reduce(s, sq, lane);
    if ((w >> 1) == g) {
#pragma unroll
      for (int t = 0; t < 4; ++t)
        *(LAS v4u*)(L + L_X1 + (tg >> 2) * 4096 + ((w & 1) * 8 + g8) * 256 + ((tg & 2) * 4 + (tg & 1)) * 16 + ((t * 32) ^ sw)) =
            v4u{pk2(x[t][0]), pk2(x[t][1]), pk2(x[t][2]), pk2(x[t][3])};
    }
    BAR();
  }
  STAMP(8);
#define GEMM32(W, XA, XB, HOOK) do { \
    v4u xf[32]; \
    _Pragma("unroll") for (int j = 0; j < 12; ++j) xf[j] = *(LAS const v4u*)(((j & 1) ? XB : XA) + (j >> 1) * 1024); \
    SCHED(); \
    _Pragma("unroll") for (int j = 0; j < 32; ++j) { \
      if (j + 12 < 32) xf[j + 12] = *(LAS const v4u*)(((j & 1) ? XB : XA) + ((j + 12) >> 1) * 1024); \
      acc[j & 1] = mfma(W[j >> 1], xf[j], acc[j & 1]); \
      SCHED(); \
      HOOK; \
    } } while (0)
  v2u resh[2], resx[2];
  {
    const unsigned s0 = (c16 ^ (2 * (c16 >> 3))) * 16, s1 = (c16 ^ (4 + 2 * (c16 >> 3))) * 16;
    LAS const char* xa = ldsp(opaque(LB + L_X + qq * 256 + s0));
    LAS const char* xb = ldsp(opaque(LB + L_X + 16384 + qq * 256 + s1));
    f32x4 acc[2] = {{0.f, 0.f, 0.f, 0.f}, {0.f, 0.f, 0.f, 0.f}};
    float mean, sd, mm[2], sdv[2];
    GEMM32(wA, xa, xb, { if (j == 1 || j == 5 || j == 9 || j == 13) W3DMA(8 + (j >> 2));
                 if (j == 20) { ln_total(part + 1024, lane, mean, sd); SCHED(); }
                 if (j == 26) { const int src = 16 * ((c16 >> 1) & 3) + 2 * (c16 >> 3) + (c16 & 1);
                                mm[0] = __shfl(mean, src); sdv[0] = __shfl(sd, src); mm[1] = __shfl(mean, src + 4); sdv[1] = __shfl(sd, src + 4); SCHED(); } });
    STAMP(28);
    unsigned d[2][2];
#pragma unroll
    for (int tt = 0; tt < 2; ++tt) {
      const float r = __builtin_amdgcn_rcpf(sdv[tt]), k1 = -2.302208198f * r, k2 = -0.1029432397f * r * r * r;
      d[tt][0] = gelu_p(fma2(sp(-mm[tt]), f32x2{cs1[0], cs1[1]}, f32x2{acc[tt][0], acc[tt][1]}), k1, k2, sdv[tt]);
      d[tt][1] = gelu_p(fma2(sp(-mm[tt]), f32x2{cs1[2], cs1[3]}, f32x2{acc[tt][2], acc[tt][3]}), k1, k2, sdv[tt]);
    }
    swap16u(d[0][0], d[1][0]); swap16u(d[0][1], d[1][1]);
    st128s(v4u{d[0][0], d[0][1], d[1][0], d[1][1]}, ws, OFF_HH + (2 * T + (qq & 1)) * 16384 + (16 * g + 2 * w + (qq >> 1)) * 256 + c16 * 16);
    SCHED();
    STAMP(29);
#pragma unroll
    for (int tt = 0; tt < 2; ++tt) {
      resh[tt] = *(LAS const v2u*)(L + L_X + tt * 16384 + (16 * g + 2 * w + (qq >> 1)) * 256 + (tt ? s1 : s0) + (qq & 1) * 8);
      resx[tt] = *(LAS const v2u*)(L + L_X1 + tt * 4096 + (2 * w + (qq >> 1)) * 256 + (tt ? s1 : s0) + (qq & 1) * 8);
    }
  }
  unsigned ho[2];
  {
    const int gk = w >> 1, shift = gk == 0 ? -1 : gk == 1 ? 1 : gk == 2 ? -2 : 2;
#pragma unroll
    for (int tt = 0; tt < 2; ++tt) {
      const int tok = 32 * T + 16 * tt + c16 + shift;
      ho[tt] = opaque((tok >= 0 && tok < 2048) ? OFF_HH + (tok >> 4) * 16384 + 2 * w * 1024 + qq * 256 + (tok & 15) * 16 : OOB);
    }
  }
  DRAIN();
  BAR();
  if (tid == 0) st32s(tag, ws, OFF_F2 + me2 * 128);
  STAMP(9);
  const int th = m >> 3, cj = m & 7;
  v4u w4[4][7];
  const unsigned w4o = OFF_W4P + (4 * cj * 56 + 7 * w) * 1024 + lane * 16;
  const f32x4 gbv = *(LAS const f32x4*)(L + L_GB + (128 * g + 16 * w + 4 * qq) * 4);
  W3DMA(12); W3DMA(13); W3DMA(14); W3DMA(15);
#pragma unroll
  for (int i = 0; i < 3; ++i) w4[2][i] = ld128(wsW4, w4o + (112 + i) * 1024, 1);
  SCHED();
  LAS char* xr = ldsp(opaque(LB + L_X + lane * 16));
  if (w == 7) {
    const int j = lane & 7, dT = j < 4 ? 0 : ((j & 1) ? 1 : -1), Tn = min(max(T + dT, 0), 63);
    poll1(ws, OFF_F2 + (Tn * 4 + (j & 3)) * 128, tag);
  }
  BAR();
  STAMP(10);
  {
    v4u xv[2][2];
#pragma unroll
    for (int tt = 0; tt < 2; ++tt) { xv[tt][0] = ld128(ws, ho[tt], 1); xv[tt][1] = ld128(ws, ho[tt] + 1024, 1); }
    SCHED();
#pragma unroll
    for (int tt = 0; tt < 2; ++tt)
#pragma unroll
      for (int i = 0; i < 2; ++i) *(LAS v4u*)(xr + tt * 16384 + (2 * w + i) * 1024) = xv[tt][i];
    BAR();
    STAMP(30);
    f32x4 acc[2] = {{0.f, 0.f, 0.f, 0.f}, {0.f, 0.f, 0.f, 0.f}};
    GEMM32(wB, xr, (xr + 16384), { if ((j & 3) == 1 && j < 28) { w4[0][j >> 2] = ld128(wsW4, w4o + (j >> 2) * 1024, 1); SCHED(); } });
    STAMP(31);
    unsigned d[2][2];
    f32x4 r[2];
    float ss[2], sq[2];
#pragma unroll
    for (int tt = 0; tt < 2; ++tt) {
      d[tt][0] = pk2(up(resh[tt].x) + f32x2{acc[tt][0], acc[tt][1]});
      d[tt][1] = pk2(up(resh[tt].y) + f32x2{acc[tt][2], acc[tt][3]});
      ss[tt] = dot2(d[tt][1], 0x3F803F80u, dot2(d[tt][0], 0x3F803F80u, 0.f));
      sq[tt] = dot2(d[tt][1], d[tt][1], dot2(d[tt][0], d[tt][0], 0.f));
    }
    unsigned e[2][2] = {{d[0][0], d[0][1]}, {d[1][0], d[1][1]}};
    swap16u(e[0][0], e[1][0]); swap16u(e[0][1], e[1][1]);
    st128s(v4u{e[0][0], e[0][1], e[1][0], e[1][1]}, ws, OFF_X1P + (2 * T + (qq & 1)) * 16384 + (16 * g + 2 * w + (qq >> 1)) * 256 + c16 * 16);
    swap32(ss[0], ss[1]); swap32(sq[0], sq[1]);
    float sa = ss[0] + ss[1], sb = sq[0] + sq[1];
    swap16(sa, sb);
    st32s(ubits(sa + sb), ws, OFF_ST + ((2 * T + (lane >> 5)) * 32 + 8 * g + w) * 128 + (lane & 31) * 4);
    SCHED();
#pragma unroll
    for (int tt = 0; tt < 2; ++tt) {
      const f32x2 r0 = fma2(f32x2{gbv[0], gbv[1]}, up(d[tt][0]), up(resx[tt].x)), r1 = fma2(f32x2{gbv[2], gbv[3]}, up(d[tt][1]), up(resx[tt].y));
      r[tt] = f32x4{r0.x, r0.y, r1.x, r1.y};
    }
    DRAIN();
    BAR();
    if (tid == 0) st32s(tag, ws, OFF_F3 + me2 * 128);
    STAMP(11);
#pragma unroll
    for (int tt = 0; tt < 2; ++tt) st128s(__builtin_bit_cast(v4u, r[tt]), ws, OFF_R + ((2 * T + tt) * 32 + 8 * g + w) * 1024 + lane * 16);
  }
#pragma unroll
  for (int i = 0; i < 7; ++i) w4[1][i] = ld128(wsW4, w4o + (56 + i) * 1024, 1);
  SCHED();
  {
    LAS char* a = ldsp(opaque(LB + L_W3 + w * 14336 + (16 * (qq >> 1) + 4 * (qq & 1) + (c16 >> 2)) * 224 + (lane & 3) * 8));
    LAS char* o = ldsp(opaque(LB + L_W3 + w * 14336 + lane * 16));
    v4u f[14];
#pragma unroll
    for (int j = 0; j < 14; ++j) {
      const v2u t0 = trread(a + (j / 7) * 7168 + (j % 7) * 32), t1 = trread(a + (j / 7) * 7168 + (j % 7) * 32 + 1792);
      f[j] = v4u{t0.x, t0.y, t1.x, t1.y};
    }
    asm volatile("s_waitcnt lgkmcnt(0)" ::: "memory");
    SCHED();
    f32x4 cs = {0.f, 0.f, 0.f, 0.f};
#pragma unroll
    for (int j = 0; j < 14; ++j) {
      *(LAS v4u*)(o + j * 1024) = f[j];
      const unsigned e = c16 == j % 7 ? 0x3F803F80u : 0u;
      cs = mfma(f[j], v4u{e, e, e, e}, cs);
    }
    *(LAS f32x4*)(L + L_X + w * 1024 + lane * 16) = cs;
  }
  if (w == 7) poll1(ws, OFF_F3 + (16 * squad + c16) * 128, tag);
  const unsigned xo4 = opaque(OFF_X1P + (8 * squad + w) * 16384 + lane * 16);
  LAS const char* w3a = ldsp(opaque(LB + L_W3 + lane * 16));
  LAS const char* w3b = ldsp(opaque(LB + L_W3 + lane * 16 + 8 * 7168));
#define W3FRAG(i, n) (*(LAS const v4u*)(((i) < 8 ? w3a : w3b) + ((i) & 7) * 7168 + (n) * 1024))
  BAR();
  STAMP(12);
  {
    v4u xv[16], sv[4], wf[2][7];
#pragma unroll
    for (int i = 0; i < 8; ++i) xv[i] = ld128(ws, xo4 + i * 1024, 1);
    SCHED();
    STAMP(32);
    f32x4 cs = {0.f, 0.f, 0.f, 0.f};
#pragma unroll
    for (int v = 0; v < 8; ++v) cs += *(LAS const f32x4*)(xr + v * 1024);
    LAS char* csw = ldsp(opaque(LB + L_X + 8192 + w * 1024 + c16 * 64 + qq * 16));
    *(LAS f32x4*)(csw) = cs;
#pragma unroll
    for (int n = 0; n < 7; ++n) wf[0][n] = W3FRAG(0, n);
    SCHED();
    f32x4 acc[7];
#pragma unroll
    for (int n = 0; n < 7; ++n) acc[n] = f32x4{0.f, 0.f, 0.f, 0.f};
#pragma unroll
    for (int i = 0; i < 16; ++i) {
      if (i < 8) xv[i + 8] = ld128(ws, xo4 + (i + 8) * 1024, 1);
      else if (i == 8) {
#pragma unroll
        for (int j = 0; j < 4; ++j) sv[j] = ld128(ws, OFF_ST + (8 * squad + w) * 4096 + j * 1024 + lane * 16, 1);
      } else if (i < 13) {
        w4[2][i - 6] = ld128(wsW4, w4o + (112 + i - 6) * 1024, 1);
      } else {
        w4[3][i - 13] = ld128(wsW4, w4o + (168 + i - 13) * 1024, 1);
      }
      SCHED();
#pragma unroll
      for (int n = 0; n < 7; ++n) {
        if (i < 15) wf[(i + 1) & 1][n] = W3FRAG(i + 1, n);
        acc[n] = mfma(wf[i & 1][n], xv[i], acc[n]);
        SCHED();
      }
    }
    STAMP(33);
    STAMP(21);
    LAS const char* csp = ldsp(opaque(LB + L_X + 8192 + w * 1024 + qq * 16));
    f32x4 c3[7];
#pragma unroll
    for (int n = 0; n < 7; ++n) c3[n] = *(LAS const f32x4*)(csp + n * 64);
    float mean, sd;
    {
      float t4[4];
#pragma unroll
      for (int j = 0; j < 4; ++j) { t4[j] = (fbits(sv[0][j]) + fbits(sv[1][j])) + (fbits(sv[2][j]) + fbits(sv[3][j])); asm("" : "+v"(t4[j])); }
      float a = t4[0], bb = t4[2], c = t4[1], dd2 = t4[3];
      swap32(a, bb); swap32(c, dd2);
      float e = a + bb, f = c + dd2;
      swap16(e, f);
      float h = e + f;
      h += ror8(h);
      const int src = ((c16 >> 1) & 1) * 32 + (c16 & 1) * 16 + (c16 >> 2);
      float s1 = __shfl(h, src), s2 = __shfl(h, src + 4);
      asm("" : "+v"(s1)); asm("" : "+v"(s2));
      ln_finish(s1, s2, mean, sd);
    }
    const float rs = __builtin_amdgcn_rcpf(sd), k1 = -2.302208198f * rs, k2 = -0.1029432397f * rs * rs * rs;
    const unsigned go = OFF_G + (8 * squad + w) * 57344 + m * 3584;
    unsigned d[7][2];
#pragma unroll
    for (int n = 0; n < 7; ++n) {
      if (n < 4) w4[3][3 + n] = ld128(wsW4, w4o + (168 + 3 + n) * 1024, 1);
      SCHED();
      d[n][0] = gelu_p(fma2(sp(-mean), f32x2{c3[n][0], c3[n][1]}, f32x2{acc[n][0], acc[n][1]}), k1, k2, sd);
      d[n][1] = gelu_p(fma2(sp(-mean), f32x2{c3[n][2], c3[n][3]}, f32x2{acc[n][2], acc[n][3]}), k1, k2, sd);
      if (n == 1 || n == 3 || n == 5) st128s(v4u{d[n - 1][0], d[n - 1][1], d[n][0], d[n][1]}, ws, go + (n >> 1) * 1024 + lane * 16);
      if (n == 6) {
        unsigned x0 = d[6][0], y0 = d[6][0], x1 = d[6][1], y1 = d[6][1];
        swap16u(x0, y0); swap16u(x1, y1);
        st128s(v4u{x0, x1, y0, y1}, ws, (qq & 1) ? OOB : go + 3072 + (qq >> 1) * 256 + c16 * 16);
      }
    }
    STAMP(13);
  }
  DRAIN();
  BAR();
  if (tid == 0) st32s(tag, ws, OFF_F4 + me2 * 128);
  STAMP(14);
  const unsigned xo = opaque(OFF_G + (8 * squad + 4 * th) * 57344 + 7 * w * 1024 + lane * 16);
  LAS char* rw0 = ldsp(opaque(LB + w * 16384 + qq * 256 + ((c16 + 4 * (qq >> 1)) & 15) * 16));
  LAS char* rw1 = ldsp(opaque(LB + w * 16384 + qq * 256 + ((c16 + 8 + 4 * (qq >> 1)) & 15) * 16));
  if (w == 7) poll1(ws, OFF_F4 + (16 * squad + c16) * 128, tag);
  BAR();
  STAMP(15);
  {
    v4u xv[2][7];
#pragma unroll
    for (int i = 0; i < 7; ++i) xv[0][i] = ld128(ws, xo + i * 1024, 1);
    const unsigned ro = OFF_R + ((8 * squad + 4 * th + (w >> 1)) * 32 + 4 * cj + 2 * (w & 1)) * 1024 + lane * 16;
    v4u r0, r1;
    SCHED();
#pragma unroll
    for (int tt = 0; tt < 4; ++tt) {
      f32x4 acc[4];
#pragma unroll
      for (int n = 0; n < 4; ++n) acc[n] = f32x4{0.f, 0.f, 0.f, 0.f};
#pragma unroll
      for (int i = 0; i < 7; ++i) {
        if (tt < 3) xv[(tt + 1) & 1][i] = ld128(ws, xo + (tt + 1) * 57344 + i * 1024, 1);
        if (tt == 3 && i == 0) { r0 = ld128(ws, ro, 1); r1 = ld128(ws, ro + 1024, 1); }
        SCHED();
#pragma unroll
        for (int n = 0; n < 4; ++n) acc[n] = mfma(w4[n][i], xv[tt & 1][i], acc[n]);
        SCHED();
      }
#pragma unroll
      for (int n = 0; n < 4; ++n) *(LAS f32x4*)(((n & 1) ? rw1 : rw0) + (tt * 4 + n) * 1024) = acc[n];
      SCHED();
    }
    *(LAS v4u*)(rw0 + (131072 - w * 14336)) = r0;
    *(LAS v4u*)(rw1 + (131072 + 1024 - w * 14336)) = r1;
    const unsigned ra = LB + ((w >> 1) * 4 + (tg >> 1)) * 1024 + (tg & 1) * 512 + ((8 * (w & 1) + g8 + 4 * tg) & 15) * 16;
    LAS const char* rr = ldsp(opaque(ra));
    LAS const char* rr2 = ldsp(opaque(ra + 65536));
    LAS const char* rr3 = ldsp(opaque(ra + 131072));
    BAR();
    STAMP(16);
    f32x4 s0 = {0.f, 0.f, 0.f, 0.f}, s1 = {0.f, 0.f, 0.f, 0.f};
#pragma unroll
    for (int v = 0; v < 8; ++v) {
      s0 += *(LAS const f32x4*)((v < 4 ? rr : rr2) + (v & 3) * 16384);
      s1 += *(LAS const f32x4*)((v < 4 ? rr : rr2) + (v & 3) * 16384 + 256);
    }
    const f32x4 q0 = *(LAS const f32x4*)(rr3), q1 = *(LAS const f32x4*)(rr3 + 256);
    const f32x4 o0 = *(LAS const f32x4*)(L + L_GB + (64 * cj + 8 * tg) * 4) * s0 + q0, o1 = *(LAS const f32x4*)(L + L_GB + (64 * cj + 8 * tg) * 4 + 16) * s1 + q1;
    const v4u o = {pk(o0[0], o0[1]), pk(o0[2], o0[3]), pk(o1[0], o1[1]), pk(o1[2], o1[3])};
    st128s(o, mkrs(p.out, 2048 * 1024), (128 * squad + 64 * th + 8 * w + g8) * 1024 + 128 * cj + 16 * tg);
  }
  if (b == 0 && tid == 0) st32s(tag, ws, OFF_EPOCH);
  STAMP(17);
  if (w == 0 && b == 100) { const unsigned long long t = __builtin_amdgcn_s_memrealtime(); while (__builtin_amdgcn_s_memrealtime() - t < (dg1 - dg0) * 4) __builtin_amdgcn_s_sleep(1); }
}
}

extern "C" void kernel_launch(void* const* d_in, const int* in_sizes, int n_in, void* d_out, int out_size, void* d_ws,
                              size_t ws_size, hipStream_t stream) {
  mk::Params p;
  p.q = (const mk::u16*)d_in[0]; p.k = (const mk::u16*)d_in[1]; p.v = (const mk::u16*)d_in[2];
  p.sk = (const mk::u16*)d_in[3]; p.ssmk = (const mk::u16*)d_in[4]; p.ssmD = (const mk::u16*)d_in[5];
  p.w1 = (const mk::u16*)d_in[7]; p.w2 = (const mk::u16*)d_in[8]; p.w3 = (const mk::u16*)d_in[9]; p.w4 = (const mk::u16*)d_in[10];
  p.c = (const mk::u16*)d_in[11]; p.wmod = (const mk::u16*)d_in[12];
  p.out = (mk::u16*)d_out; p.ws = (char*)d_ws;
  mk::mega<<<256, 512, 0, stream>>>(p);
}
```

```cpp
#include <hip/hip_runtime.h>
#include <stdint.h>

namespace mk {
typedef unsigned short u16;
typedef unsigned v4u __attribute__((ext_vector_type(4)));
typedef unsigned v2u __attribute__((ext_vector_type(2)));
typedef float f32x4 __attribute__((ext_vector_type(4)));
typedef float f32x2 __attribute__((ext_vector_type(2)));
typedef __bf16 bf16x8 __attribute__((ext_vector_type(8)));
typedef __bf16 bf16x2 __attribute__((ext_vector_type(2)));
typedef short s16x4 __attribute__((ext_vector_type(4)));
typedef __amdgpu_buffer_rsrc_t rsrc_t;
#define LAS __attribute__((address_space(3)))
#define GAS __attribute__((address_space(1)))
#define DEVI __device__ __forceinline__

constexpr int SC1 = 16;
constexpr int OFF_EPOCH = 0;
constexpr int OFF_F0 = 4096;
constexpr int OFF_FR = OFF_F0 + 1024 * 128;
constexpr int OFF_F1 = OFF_FR + 8 * 8192;
constexpr int OFF_F2 = OFF_F1 + 8 * 8192;
constexpr int OFF_F3 = OFF_F2 + 256 * 128;
constexpr int OFF_F4 = OFF_F3 + 256 * 128;
constexpr int OFF_FW = OFF_F4 + 256 * 128;
constexpr int OFF_MOD = OFF_FW + 128 * 128;
constexpr int OFF_ST = 512 << 10;
constexpr int OFF_UR = 1 << 20;
constexpr int OFF_Z = OFF_UR + (2 << 20);
constexpr int OFF_W1P = OFF_Z + (2 << 20);
constexpr int OFF_W2P = OFF_W1P + (512 << 10);
constexpr int OFF_W4P = OFF_W2P + (512 << 10);
constexpr int OFF_HH = OFF_W4P + (2 << 20);
constexpr int OFF_X1P = OFF_HH + (2 << 20);
constexpr int OFF_R = OFF_X1P + (2 << 20);
constexpr int OFF_G = OFF_R + (4 << 20);
constexpr int WS_USED = OFF_G + 128 * 57344;
static_assert(OFF_MOD + 49152 <= OFF_ST, "ws");
constexpr unsigned OOB = 0x80000000u;
constexpr int L_W3 = 0;
constexpr int L_KC = 112256;
constexpr int L_UC = L_KC + 8 * 4160;
constexpr int L_X = 114688;
constexpr int L_X1 = L_X + 32768;
constexpr int L_PART = L_X1 + 8192;
constexpr int L_GB = L_PART;
constexpr int L_MV = 0;
constexpr int L_RED = 78848;
constexpr int L_KT = 0;
constexpr int L_VT = L_KT + 9 * 1040;
constexpr int L_STG = 17920;
constexpr int L_CNT = L_STG + 9216;
constexpr int L_S1 = L_CNT + 64;
constexpr int L_Q = L_S1 + 256;
constexpr int L_WM = L_Q + 32 * 1088;
constexpr int L_SC = L_KT;
static_assert(L_VT + 8 * 1040 <= L_STG && L_WM + 16384 <= L_RED, "lds");
static_assert(L_UC + 4 * 4576 == 163840, "lds");
static_assert(L_PART + 3 * 2048 <= 163840, "lds");

#define STAMP(i) do {} while (0)

struct Params {
  const u16 *q, *k, *v, *sk, *ssmk, *ssmD, *w1, *w2, *w3, *w4, *c, *wmod;
  u16* out;
  char* ws;
};

DEVI float lo(unsigned u) { return __builtin_bit_cast(float, u << 16); }
DEVI float hi(unsigned u) { return __builtin_bit_cast(float, u & 0xffff0000u); }
DEVI unsigned pk(float a, float b) { f32x2 f = {a, b}; return __builtin_bit_cast(unsigned, __builtin_convertvector(f, bf16x2)); }
DEVI float rb(float a) { return lo(pk(a, a)); }
DEVI f32x4 mfma(v4u a, v4u b, f32x4 c) {
  return __builtin_amdgcn_mfma_f32_16x16x32_bf16(__builtin_bit_cast(bf16x8, a), __builtin_bit_cast(bf16x8, b), c, 0, 0, 0);
}
DEVI rsrc_t mkrs(const void* p, int bytes) { return __builtin_amdgcn_make_buffer_rsrc((void*)p, 0, bytes, 0x00020000); }
#define NTAUX 2
DEVI v4u ld128(rsrc_t r, unsigned off, int aux) { return aux ? __builtin_amdgcn_raw_buffer_load_b128(r, off, 0, SC1) : __builtin_amdgcn_raw_buffer_load_b128(r, off, 0, NTAUX); }
DEVI v4u ld128so(rsrc_t r, unsigned off, unsigned so) { return __builtin_amdgcn_raw_buffer_load_b128(r, off, so, SC1); }
DEVI v2u ld64s(rsrc_t r, unsigned off) { return __builtin_amdgcn_raw_buffer_load_b64(r, off, 0, SC1); }
DEVI unsigned ld32(rsrc_t r, unsigned off, int aux) { return aux ? __builtin_amdgcn_raw_buffer_load_b32(r, off, 0, SC1) : __builtin_amdgcn_raw_buffer_load_b32(r, off, 0, NTAUX); }
DEVI void st128s(v4u v, rsrc_t r, unsigned off) { __builtin_amdgcn_raw_buffer_store_b128(v, r, off, 0, SC1); }
DEVI void st32s(unsigned v, rsrc_t r, unsigned off) { __builtin_amdgcn_raw_buffer_store_b32(v, r, off, 0, SC1); }
#define MEMFENCE() asm volatile("" ::: "memory")
#define DRAIN() asm volatile("s_waitcnt vmcnt(0)" ::: "memory")
#define BAR() asm volatile("s_waitcnt lgkmcnt(0)\n\ts_barrier" ::: "memory")
#define SCHED() __builtin_amdgcn_sched_barrier(0)
constexpr unsigned kSpinCap = 1u << 21;

DEVI void poll1(rsrc_t r, unsigned off, unsigned tag) {
  for (unsigned s = 0; s < kSpinCap; ++s) {
    MEMFENCE();
    const unsigned f = ld32(r, off, 1);
    if (__all(f == tag)) break;
  }
  MEMFENCE();
}
DEVI void poll4(rsrc_t r, unsigned off, unsigned tag) {
  for (unsigned s = 0; s < kSpinCap; ++s) {
    MEMFENCE();
    const v4u f = ld128(r, off, 1);
    if (__all((f.x == tag) & (f.y == tag) & (f.z == tag) & (f.w == tag))) break;
  }
  MEMFENCE();
}
DEVI float fbits(unsigned u) { return __builtin_bit_cast(float, u); }
DEVI unsigned ubits(float f) { return __builtin_bit_cast(unsigned, f); }
DEVI float ror8(float x) { return fbits(__builtin_amdgcn_update_dpp(0u, ubits(x), 0x128, 0xf, 0xf, false)); }
DEVI void swap32(float& a, float& b) { auto r = __builtin_amdgcn_permlane32_swap(ubits(a), ubits(b), false, false); a = fbits(r[0]); b = fbits(r[1]); }
DEVI void swap16(float& a, float& b) { auto r = __builtin_amdgcn_permlane16_swap(ubits(a), ubits(b), false, false); a = fbits(r[0]); b = fbits(r[1]); }
DEVI void swap16u(unsigned& a, unsigned& b) { auto r = __builtin_amdgcn_permlane16_swap(a, b, false, false); a = r[0]; b = r[1]; }

DEVI float ln_wave_reduce(const float (&s)[4], const float (&qv)[4], int lane) {
  float a0 = s[0], b0 = s[2], a1 = s[1], b1 = s[3], c0 = qv[0], d0 = qv[2], c1 = qv[1], d1 = qv[3];
  swap32(a0, b0); swap32(a1, b1); swap32(c0, d0); swap32(c1, d1);
  float s0 = a0 + b0, s1 = a1 + b1, q0 = c0 + d0, q1 = c1 + d1;
  swap16(s0, s1); swap16(q0, q1);
  const float x = s0 + s1, y = q0 + q1;
  const bool b3 = lane & 8;
  return (b3 ? y : x) + ror8(b3 ? x : y);
}
DEVI void ln_finish(float sum, float sq, float& mean, float& sd) {
  const float m = sum * (1.f / 512.f);
  const float var = fmaxf(sq * (1.f / 512.f) - m * m, 0.f);
  mean = rb(m);
  sd = rb(__builtin_amdgcn_sqrtf(rb(rb(var) + 1e-5f)));
}
DEVI void ln_total(LAS const float* part, int lane, float& mean, float& sd) {
  float t = 0.f;
#pragma unroll
  for (int w = 0; w < 8; ++w) t += part[w * 64];
  const float o = ror8(t);
  const bool b3 = lane & 8;
  ln_finish(b3 ? o : t, b3 ? t : o, mean, sd);
}
DEVI void gather4(float x, float (&o)[4]) {
  float e = x, d = x;
  swap16(e, d);
  float e2 = e, d2 = d;
  swap32(e, e2); swap32(d, d2);
  o[0] = e; o[2] = e2; o[1] = d; o[3] = d2;
}
DEVI float gelu(float x) {
  const float t = x * (2.302208198f + 0.1029432397f * x * x);
  return x * __builtin_amdgcn_rcpf(1.f + __builtin_amdgcn_exp2f(-t));
}
DEVI void glds16(const void* gsrc, unsigned lds_dst) {
  unsigned keep;
  asm volatile("s_mov_b32 %0, m0\n\ts_mov_b32 m0, %2\n\ts_nop 0\n\tglobal_load_lds_dwordx4 %1, off\n\ts_mov_b32 m0, %0" : "=&s"(keep) : "v"(gsrc), "s"(lds_dst) : "memory");
}
DEVI void glds16s(unsigned voff, const void* sbase, unsigned lds_dst) {
  unsigned keep;
  asm volatile("s_mov_b32 %0, m0\n\ts_mov_b32 m0, %3\n\ts_nop 0\n\tglobal_load_lds_dwordx4 %1, %2\n\ts_mov_b32 m0, %0" : "=&s"(keep) : "v"(voff), "s"(sbase), "s"(lds_dst) : "memory");
}
DEVI void glds16nt(unsigned voff, const void* sbase, unsigned lds_dst) {
  unsigned keep;
  asm volatile("s_mov_b32 %0, m0\n\ts_mov_b32 m0, %3\n\ts_nop 0\n\tglobal_load_lds_dwordx4 %1, %2 nt\n\ts_mov_b32 m0, %0" : "=&s"(keep) : "v"(voff), "s"(sbase), "s"(lds_dst) : "memory");
}
#define CODEB 30
constexpr int CODE_BYTES = CODEB * 1024;
DEVI v2u trread(LAS char* a) { return __builtin_bit_cast(v2u, __builtin_amdgcn_ds_read_tr16_b64_v4i16((LAS s16x4*)a)); }

DEVI f32x2 up(unsigned u) { return f32x2{lo(u), hi(u)}; }
DEVI unsigned pk2(f32x2 f) { return __builtin_bit_cast(unsigned, __builtin_convertvector(f, bf16x2)); }
DEVI f32x2 fma2(f32x2 a, f32x2 b, f32x2 c) { return __builtin_elementwise_fma(a, b, c); }
DEVI f32x2 sp(float a) { return f32x2{a, a}; }
DEVI unsigned gelu2(f32x2 x) {
  const f32x2 t = x * fma2(x * x, sp(-0.1029432397f), sp(-2.302208198f));
  const f32x2 e = f32x2{__builtin_amdgcn_exp2f(t.x), __builtin_amdgcn_exp2f(t.y)} + sp(1.f);
  return pk2(x * f32x2{__builtin_amdgcn_rcpf(e.x), __builtin_amdgcn_rcpf(e.y)});
}
DEVI float dot2(unsigned a, unsigned b, float c) {
  return __builtin_amdgcn_fdot2_f32_bf16(__builtin_bit_cast(bf16x2, a), __builtin_bit_cast(bf16x2, b), c, false);
}
DEVI float gelu_s(float a, float k1, float k2, float sd) {
  const float e = __builtin_amdgcn_exp2f(a * __builtin_fmaf(a * a, k2, k1));
  float o = a * __builtin_amdgcn_rcpf(__builtin_fmaf(e, sd, sd));
  asm("" : "+v"(o));
  return o;
}
DEVI unsigned gelu_p(f32x2 a, float k1, float k2, float sd) {
  const f32x2 t = a * fma2(a * a, sp(k2), sp(k1));
  const f32x2 d = fma2(f32x2{__builtin_amdgcn_exp2f(t.x), __builtin_amdgcn_exp2f(t.y)}, sp(sd), sp(sd));
  return pk2(a * f32x2{__builtin_amdgcn_rcpf(d.x), __builtin_amdgcn_rcpf(d.y)});
}
template <int CTRL> DEVI float dppf(float x) { return fbits(__builtin_amdgcn_update_dpp(0u, ubits(x), CTRL, 0xf, 0xf, false)); }
DEVI void lds_wait(LAS char* L, int word, unsigned want) {
#pragma nounroll
  for (unsigned s = 0; s < (1u << 26); ++s) {
    if (*(volatile LAS unsigned*)(L + L_CNT + 4 * word) >= want) break;
    __builtin_amdgcn_s_sleep(1);
  }
  MEMFENCE();
}
DEVI unsigned lds_inc(LAS char* L, int word, int lane) {
  unsigned old = 0;
  asm volatile("s_waitcnt lgkmcnt(0)" ::: "memory");
  if (lane == 0) old = __hip_atomic_fetch_add((LAS unsigned*)(L + L_CNT + 4 * word), 1u, __ATOMIC_RELAXED, __HIP_MEMORY_SCOPE_WORKGROUP);
  return __builtin_amdgcn_readfirstlane(old);
}
template <typename T> DEVI T opaque(T x) { asm volatile("" : "+v"(x)); return x; }
DEVI LAS char* ldsp(unsigned a) { return (LAS char*)(size_t)a; }

__global__ void __launch_bounds__(512, 2) mega(Params p) {
  __shared__ __attribute__((aligned(16))) char smem[163840];
  LAS char* L = (LAS char*)smem;
  const unsigned LB = (unsigned)(size_t)L;
  STAMP(0);
  unsigned long long pc;
  asm volatile("s_getpc_b64 %0" : "=s"(pc));
  const int tid = threadIdx.x, lane = tid & 63, w = __builtin_amdgcn_readfirstlane(tid >> 6);
  const int b = blockIdx.x, team = b & 7, rank = b >> 3, me = team * 32 + rank;
  const int c16 = lane & 15, qq = lane >> 4;
#define MAP2D 0
  const int squad = MAP2D ? 4 * (team >> 1) + (rank >> 3) : me >> 4, m = MAP2D ? 8 * (team & 1) + (rank & 7) : me & 15;
  const int T = MAP2D ? 4 * squad + ((rank & 7) >> 1) : me >> 2, g = MAP2D ? 2 * (team & 1) + (rank & 1) : me & 3, me2 = 4 * T + g;
  const int g8 = lane >> 3, tg = lane & 7;
  const rsrc_t ws = mkrs(p.ws, WS_USED);
#define BGOFF 0
  const rsrc_t wsW12 = mkrs(p.ws, (BGOFF & 1) ? 0 : WS_USED), wsW4 = mkrs(p.ws, (BGOFF & 2) ? 0 : WS_USED), wsRT = mkrs(p.ws, (BGOFF & 4) ? 0 : WS_USED);

  const unsigned wo = ((8 * g + w) * 16) * 1024 + lane * 16;
  const unsigned tile_o = (64 * rank + g8) * 1024 + team * 128 + tg * 16;
  v4u wA[16], wB[16];
  unsigned tag;
#define RETILE(DST, STR) do { \
    LAS char* stg = L + L_STG; \
    _Pragma("unroll") for (int j = 0; j < 8; ++j) *(LAS v4u*)(stg + (qq * 8 + j) * 288 + c16 * 16) = raw[j]; \
    asm volatile("s_waitcnt lgkmcnt(0)" ::: "memory"); \
    _Pragma("unroll") for (int i = 0; i < 8; ++i) { \
      LAS char* a = stg + (qq * 8 + (c16 >> 2)) * 288 + i * 32 + (lane & 3) * 8; \
      const v2u t0 = trread(a), t1 = trread(a + 4 * 288); \
      __builtin_amdgcn_raw_buffer_store_b128(v4u{t0.x, t0.y, t1.x, t1.y}, wsRT, lane * 16, (DST) + i * (STR), SC1); \
    } } while (0)
  if (w < 4) {
    const unsigned vo = tile_o + w * 16384;
    glds16nt(vo, p.k, LB + L_KT + (2 * w + 1) * 1040);
    glds16nt(vo, p.v, LB + L_VT + (2 * w) * 1040);
    glds16nt(vo + 8192, p.k, LB + L_KT + (2 * w + 2) * 1040);
    glds16nt(vo + 8192, p.v, LB + L_VT + (2 * w + 1) * 1040);
    if (w == 0) glds16nt(rank ? vo - 8192 : vo, p.k, LB + L_KT);
  }
  const v4u sk = *(const GAS v4u*)(p.sk + (64 * team + 2 * (8 * (w & 3) + g8)) * 4);
  const unsigned epoch = ld32(ws, OFF_EPOCH, 1);
  if (tid >= 504) *(LAS unsigned*)(L + L_CNT + (tid & 7) * 4) = 0u;
  BAR();
#define NWB 8
  const v4u ones = {0x3F803F80u, 0x3F803F80u, 0x3F803F80u, 0x3F803F80u};
  v4u kp[4], raw5[8];
  if (w >= 4) {
    const rsrc_t rssm = mkrs(p.ssmk, (BGOFF & 64) ? 0 : 512 * 2048 * 2);
    const int a = tid & 255;
    const unsigned o = (unsigned)(2 * me * 4096 + a * 16);
    kp[1] = ld128(rssm, o, 0); kp[3] = ld128(rssm, o + 4096, 0);
    kp[0] = ld128(rssm, a ? o - 16 : OOB, 0); kp[2] = ld128(rssm, a ? o + 4080 : OOB, 0);
    if (w == 5) {
      const int u = b >> 1;
      const rsrc_t rw = mkrs(u < 64 ? p.w1 : p.w2, ((BGOFF & 4) || (b & 1)) ? 0 : 512 * 1024);
#pragma unroll
      for (int j = 0; j < 8; ++j) raw5[j] = ld128(rw, (unsigned)((32 * (u & 15) + 8 * qq) * 1024 + 256 * ((u >> 4) & 3) + 16 * c16) + j * 1024, 0);
    }
    if (w == 4) raw5[0] = *(const GAS v4u*)(p.c + 8 * lane);
    if (w == 7) {
      const int j = lane & 15;
      unsigned fo = OFF_F0 + ((team * 4 + j) * 32 + rank) * 128;
      fo = j >= 4 ? OFF_FR + (j & 1) * 8 * 8192 + ((b & 3) * 2 + ((j >> 1) & 1)) * 8192 + (b >> 2) * 128 : fo;
      fo = j >= 8 ? OFF_F2 + (j - 8) * 32768 + me2 * 128 : fo;
      fo = j >= 11 ? OFF_FW + (b >> 1) * 128 : fo;
      const unsigned x = ld32(ws, fo, 1);
      const unsigned y = *(const GAS unsigned*)((pc & ~255ull) + (size_t)min(rank * 2048 + lane * 32, CODE_BYTES));
      asm volatile("" ::"v"(x), "v"(y));
    }
  }
  SCHED();
  if (__builtin_expect(w < 4, 1)) {
    LAS const char* kt = ldsp(opaque(LB + L_KT + tg * 1040 + (8 * w + g8) * 4));
    const unsigned uo = opaque(OFF_UR + (64 * team + 2 * (8 * w + g8)) * 4096 + (2040 - 64 * rank - 8 * tg) * 2);
    __builtin_amdgcn_s_setprio(3);
    DRAIN();
    if (lane == 0) __hip_atomic_fetch_add((LAS unsigned*)(L + L_CNT + 20), 1u, __ATOMIC_RELAXED, __HIP_MEMORY_SCOPE_WORKGROUP);
    for (unsigned s = 0; s < (1u << 26); ++s)
      if (*(volatile LAS unsigned*)(L + L_CNT + 20) >= 4) break;
    MEMFENCE();
    STAMP(24);
    {
      unsigned kk[11], vv[8];
#pragma unroll
      for (int i = 0; i < 3; ++i) kk[i] = *(LAS const unsigned*)(kt + 640 + 128 * i);
#pragma unroll
      for (int i = 0; i < 8; ++i) { kk[i + 3] = *(LAS const unsigned*)(kt + 1040 + 128 * i); vv[i] = *(LAS const unsigned*)(kt + L_VT + 128 * i); }
      if (rank == 0 && tg == 0) { kk[0] = 0u; kk[1] = 0u; kk[2] = 0u; }
      const f32x2 a0 = {lo(sk.x), lo(sk.z)}, a1 = {hi(sk.x), hi(sk.z)}, a2 = {lo(sk.y), lo(sk.w)}, a3 = {hi(sk.y), hi(sk.w)};
      f32x2 kf[11], x[8];
#pragma unroll
      for (int i = 0; i < 11; ++i) kf[i] = up(kk[i]);
#pragma unroll
      for (int t = 0; t < 8; ++t) x[t] = fma2(kf[t + 3], a0, fma2(kf[t + 2], a1, fma2(kf[t + 1], a2, kf[t] * a3))) * up(vv[t]);
      st128s(v4u{pk(x[7].x, x[6].x), pk(x[5].x, x[4].x), pk(x[3].x, x[2].x), pk(x[1].x, x[0].x)}, ws, uo);
      st128s(v4u{pk(x[7].y, x[6].y), pk(x[5].y, x[4].y), pk(x[3].y, x[2].y), pk(x[1].y, x[0].y)}, ws, uo + 4096);
    }
    MEMFENCE();
    if (lane == 0) __hip_atomic_fetch_add((LAS unsigned*)(L + L_CNT + 24), 1u, __ATOMIC_RELAXED, __HIP_MEMORY_SCOPE_WORKGROUP);
    SCHED();
    STAMP(22);
    tag = __builtin_amdgcn_readfirstlane(epoch * 0x9E3779B1u + 0x7F4A7C15u);
    const unsigned o = opaque(OFF_UR + (2 * me + (w >> 1)) * 4096 + lane * 16);
    LAS char* uc = ldsp(opaque(LB + L_UC + (w >> 1) * 2 * 4576 + lane * 16));
    LAS const char* pa = ldsp(opaque(LB + L_KC + ((w >> 1) * 4 + (c16 & 3)) * 4160 + (qq + (c16 >> 2)) * 16 + (w & 1) * 256));
    LAS const char* pb = ldsp(opaque(LB + L_UC + ((w >> 1) * 2 + (c16 & 1)) * 4576 + (7 - (c16 >> 1)) * 64 + qq * 16 + (w & 1) * 256));
    LAS char* rp = ldsp(opaque(LB + L_RED + w * 8192 + (8 * (c16 >> 1) + ((2 * qq + (c16 & 1)) ^ (c16 & 6))) * 16));
    DRAIN();
    if (lane == 0) st32s(tag, ws, OFF_F0 + ((team * 4 + w) * 32 + rank) * 128);
    STAMP(1);
    lds_wait(L, 0, 4);
    if (w == 0) {
      poll1(ws, OFF_F0 + ((team * 4 + (rank >> 3)) * 32 + (lane & 31)) * 128, tag);
      if (lane == 0) *(volatile LAS unsigned*)(L + L_CNT + 8) = 1u;
    } else {
      lds_wait(L, 2, 1);
    }
    STAMP(2);
    v4u fa[2][8], fb[2][8];
    {
      v4u u1[4]; unsigned nx[4];
#pragma unroll
      for (int j = 0; j < 4; ++j) { u1[j] = ld128(ws, o + j * 1024, 1); nx[j] = ld32(ws, (j == 3 && lane == 63) ? OOB : o + j * 1024 + 16, 1); }
#pragma unroll
      for (int i = 0; i < 8; ++i) fa[0][i] = *(LAS const v4u*)(pa + 512 * i);
      SCHED();
#pragma unroll
      for (int j = 0; j < 4; ++j) {
        *(LAS v4u*)(uc + 4576 + j * 1024) = u1[j];
        *(LAS v4u*)(uc + j * 1024) = v4u{__builtin_amdgcn_alignbit(u1[j].y, u1[j].x, 16), __builtin_amdgcn_alignbit(u1[j].z, u1[j].y, 16),
                                         __builtin_amdgcn_alignbit(u1[j].w, u1[j].z, 16), __builtin_amdgcn_alignbit(nx[j], u1[j].w, 16)};
      }
      if (lane == 0) __hip_atomic_fetch_add((LAS unsigned*)(L + L_CNT + 12), 1u, __ATOMIC_RELAXED, __HIP_MEMORY_SCOPE_WORKGROUP);
      MEMFENCE();
    }
    STAMP(3);
    f32x4 cacc[8];
#pragma unroll
    for (int gi = 0; gi < 8; ++gi) cacc[gi] = f32x4{0.f, 0.f, 0.f, 0.f};
#pragma unroll
    for (int i = 0; i < 8; ++i) fb[0][i] = *(LAS const v4u*)(pb + 512 * (7 - i));
    SCHED();
#pragma unroll
    for (int t = 0; t < 4; ++t) {
      if (t < 3) {
        fa[(t + 1) & 1][0] = *(LAS const v4u*)(pa + 64 * (t + 1));
#pragma unroll
        for (int i = 0; i < 8; ++i) fb[(t + 1) & 1][i] = *(LAS const v4u*)(pb + 64 * (t + 1) + 512 * (7 - i));
#pragma unroll
        for (int i = 1; i < 8; ++i) fa[(t + 1) & 1][i] = *(LAS const v4u*)(pa + 64 * (t + 1) + 512 * i);
      }
#pragma unroll
      for (int i = 0; i < 8; ++i) {
#pragma unroll
        for (int n = 0; n + i < 8; ++n) cacc[i + n] = mfma(fa[t & 1][i], fb[t & 1][n], cacc[i + n]);
        if (t == 3) {
          if (i > 0) *(LAS f32x4*)(rp + (i - 1) * 1024) = cacc[i - 1];
          SCHED();
        }
      }
      if (t < 3) {
        __builtin_amdgcn_sched_group_barrier(0x8, 4, 0);
#pragma unroll
        for (int i = 0; i < 16; ++i) {
          __builtin_amdgcn_sched_group_barrier(0x100, 1, 0);
          __builtin_amdgcn_sched_group_barrier(0x8, 2, 0);
        }
      }
      SCHED();
    }
    STAMP(23);
    *(LAS f32x4*)(rp + 7 * 1024) = cacc[7];
    __builtin_amdgcn_s_setprio(0);
  } else {
    v4u raw[8];
    const bool dow4 = w == 5 && b < 224;
    const int cb4 = b / 56, ks4 = b % 56, P4 = 4 * ks4 + qq, sl4 = P4 / 14, pp4 = P4 % 14;
    const int k04 = 112 * sl4 + (pp4 < 12 ? 32 * (pp4 >> 2) + 4 * (pp4 & 3) : 96 + 8 * (pp4 - 12));
    const unsigned o0 = opaque(k04 * 1024 + 256 * cb4 + 16 * c16), o1 = opaque(o0 + (pp4 < 12 ? 16 : 4) * 1024);
    {
      const v4u z4 = {0u, 0u, 0u, 0u};
      if (w == 4 && (lane & 3) < 3 && lane < 32) *(LAS v4u*)(L + L_KC + (lane >> 2) * 4160 + (lane & 3) * 16) = z4;
      if (w >= 6 && (lane & 31) < 30) *(LAS v4u*)(L + L_UC + ((w & 1) * 2 + (lane >> 5)) * 4576 + (256 + (lane & 31)) * 16) = z4;
      LAS char* kc = L + L_KC + ((tid & 255) + 3) * 16;
#pragma unroll
      for (int ch = 0; ch < 2; ++ch) {
        const v4u k0 = kp[2 * ch], k1 = kp[2 * ch + 1];
        *(LAS v4u*)(kc + (4 * ch + 0) * 4160) = v4u{k0.y, k0.z, k0.w, k1.x};
        *(LAS v4u*)(kc + (4 * ch + 1) * 4160) = v4u{k0.z, k0.w, k1.x, k1.y};
        *(LAS v4u*)(kc + (4 * ch + 2) * 4160) = v4u{k0.w, k1.x, k1.y, k1.z};
        *(LAS v4u*)(kc + (4 * ch + 3) * 4160) = k1;
      }
      lds_inc(L, 0, lane);
    }
    DRAIN();
    tag = __builtin_amdgcn_readfirstlane(epoch * 0x9E3779B1u + 0x7F4A7C15u);
    STAMP(25);
    if (w == 5) {
      if ((b & 1) == 0) {
        const int u = b >> 1;
#pragma unroll
        for (int j = 0; j < 8; ++j) raw[j] = raw5[j];
        RETILE(OFF_W1P + ((u >> 4) * 8 * 16 + (u & 15)) * 1024, 16 * 1024);
        DRAIN();
        if (lane == 0) st32s(tag, ws, OFF_FW + u * 128);
      }
      if (dow4) {
        const rsrc_t rw = mkrs(p.w4, (BGOFF & 4) ? 0 : 1792 * 1024);
        lds_wait(L, 6, 4);
#pragma unroll
        for (int j = 0; j < 8; ++j) raw[j] = ld128(rw, (j < 4 ? o0 : o1) + (j & 3) * 1024, 0);
        lds_wait(L, 3, 4);
        RETILE(OFF_W4P + (cb4 * 8 * 56 + ks4) * 1024, 56 * 1024);
      }
    } else if (w == 4) {
      v4u sv;
#pragma unroll
      for (int j = 0; j < 4; ++j) {
        const f32x2 x = up(raw5[0][j]);
        sv[j] = pk(x.x * __builtin_amdgcn_rcpf(1.f + __builtin_amdgcn_exp2f(-1.442695041f * x.x)),
                   x.y * __builtin_amdgcn_rcpf(1.f + __builtin_amdgcn_exp2f(-1.442695041f * x.y)));
      }
      lds_wait(L, 6, 4);
      STAMP(26);
      if (b < 192) {
        const int kq = b & 3, cb = b >> 2;
        if (!(BGOFF & 8)) {
#pragma unroll
          for (int i = 0; i < 16; ++i)
            glds16nt(g8 * 6144 + tg * 16, (const char*)p.wmod + (128 * kq + 8 * i) * 6144 + cb * 128, LB + L_WM + i * 1024);
        }
        STAMP(27);
        *(LAS v4u*)(L + L_SC + lane * 16) = sv;
        v4u sf[4];
#pragma unroll
        for (int i = 0; i < 4; ++i) sf[i] = *(LAS const v4u*)(L + L_SC + kq * 256 + i * 64 + qq * 16);
        LAS char* a = ldsp(opaque(LB + L_WM + qq * 1024 + (c16 >> 2) * 128 + (lane & 3) * 8));
        DRAIN();
        STAMP(34);
        f32x4 acc[4];
#pragma unroll
        for (int n = 0; n < 4; ++n) {
          acc[n] = f32x4{0.f, 0.f, 0.f, 0.f};
#pragma unroll
          for (int i = 0; i < 4; ++i) {
            const v2u t0 = trread(a + i * 4096 + n * 32), t1 = trread(a + i * 4096 + n * 32 + 512);
            acc[n] = mfma(v4u{t0.x, t0.y, t1.x, t1.y}, sf[i], acc[n]);
          }
        }
        const f32x4 r = c16 == 0 ? acc[0] : c16 == 1 ? acc[1] : c16 == 2 ? acc[2] : acc[3];
        st128s(__builtin_bit_cast(v4u, r), ws, c16 < 4 ? OFF_MOD + kq * 12288 + cb * 256 + c16 * 64 + qq * 16 : OOB);
        DRAIN();
      }
      if (lane < 8) st32s(tag, ws, OFF_FR + lane * 8192 + (b >> 2) * 128 + (b & 3) * 4);
    }
    STAMP(18);
    if (w == 7) {
      lds_wait(L, 2, 1);
      poll1(ws, OFF_FW + ((lane & 16) * 4 + 16 * g + c16) * 128, tag);
      lds_inc(L, 3, lane);
    }
    lds_wait(L, 3, 5);
    STAMP(19);
    if (w >= 6 && !(BGOFF & 16)) {
#pragma unroll
      for (int j = 0; j < 16; ++j)
        glds16nt(lane * 16, (const char*)p.q + (32 * T + 8 * ((j & 7) >> 1) + (j & 1) + 2 * (2 * (w & 1) + (j >> 3))) * 1024, LB + L_Q + (16 * (w & 1) + j) * 1088);
    }
#pragma unroll
    for (int i = 0; i < 16; ++i) wA[i] = ld128(wsW12, OFF_W1P + wo + i * 1024, 1);
#pragma unroll
    for (int i = 0; i < NWB; ++i) wB[i] = ld128(wsW12, OFF_W2P + wo + i * 1024, 1);
    SCHED();
    STAMP(20);
  }
  const unsigned dd = *(const __attribute__((address_space(4))) unsigned*)(p.ssmD + 2 * me);
  LAS const char* rq = ldsp(opaque(LB + L_RED + w * 1024 + (lane ^ (2 * (g8 & 3))) * 16));
  LAS const char* uq = ldsp(opaque(LB + L_UC + 4576 + (2040 - (256 * w + 32 * g8 + 8 * (tg >> 1))) * 2));
  BAR();
  STAMP(4);
  {
    const f32x4 y0 = *(LAS const f32x4*)(rq) + *(LAS const f32x4*)(rq + 8192);
    const f32x4 y1 = *(LAS const f32x4*)(rq + 16384) + *(LAS const f32x4*)(rq + 24576);
    const v4u k0 = *(LAS const v4u*)(uq), k1 = *(LAS const v4u*)(uq + 2 * 4576);
    const unsigned d0 = (tg & 1) ? (dd & 0xffffu) : (dd << 16), d1 = (tg & 1) ? (dd >> 16) : (dd & 0xffff0000u);
    v4u o;
#pragma unroll
    for (int t = 0; t < 4; ++t) o[t] = pk(dot2(k0[3 - t], d0, y0[t]), dot2(k1[3 - t], d1, y1[t]));
    st128s(o, ws, OFF_Z + (8 * w + g8) * 32768 + me * 128 + tg * 16);
  }
  if (w == 4) poll4(ws, OFF_FR + team * 8192 + lane * 128, tag);
  DRAIN();
  BAR();
  if (tid < 8) st32s(tag, ws, OFF_F1 + tid * 8192 + (b >> 2) * 128 + (b & 3) * 4);
  STAMP(5);

  if (w >= 6) {
    LAS const char* a = ldsp(opaque(LB + L_Q + (16 * (w & 1) + c16) * 1088 + qq * 16));
    f32x4 aS = {0.f, 0.f, 0.f, 0.f}, aQ = {0.f, 0.f, 0.f, 0.f};
    v4u xf[16];
#pragma unroll
    for (int i = 0; i < 16; ++i) xf[i] = *(LAS const v4u*)(a + i * 64);
#pragma unroll
    for (int i = 0; i < 16; ++i) { aS = mfma(ones, xf[i], aS); aQ = mfma(xf[i], xf[i], aQ); }
    const int r3 = c16 & 3;
    float mean, sd;
    ln_finish(aS[0], r3 == 0 ? aQ[0] : r3 == 1 ? aQ[1] : r3 == 2 ? aQ[2] : aQ[3], mean, sd);
    const float rs = __builtin_amdgcn_rcpf(sd);
    if (qq == (c16 >> 2)) *(LAS f32x2*)(L + L_S1 + (c16 & 7) * 32 + (2 * (w & 1) + (c16 >> 3)) * 8) = f32x2{-mean * rs, rs};
  } else {
    v4u pv[2][4];
#pragma unroll
    for (int h = 0; h < 2; ++h)
#pragma unroll
      for (int k = 0; k < 4; ++k) pv[h][k] = ld128(ws, OFF_MOD + k * 12288 + w * 2048 + h * 1024 + lane * 16, 1);
    SCHED();
    const float one = (w == 1 || w == 4) ? 1.f : 0.f;
#pragma unroll
    for (int h = 0; h < 2; ++h) {
      f32x4 v = (__builtin_bit_cast(f32x4, pv[h][0]) + __builtin_bit_cast(f32x4, pv[h][1])) + (__builtin_bit_cast(f32x4, pv[h][2]) + __builtin_bit_cast(f32x4, pv[h][3]));
      const f32x2 a = up(pk2(up(pk(v[0], v[1])) + sp(one))), c = up(pk2(up(pk(v[2], v[3])) + sp(one)));
      *(LAS f32x4*)(L + (w == 5 ? L_GB : L_MV + w * 2048) + h * 1024 + lane * 16) = f32x4{a.x, a.y, c.x, c.y};
    }
  }
  unsigned ghl[4][4], ghh[4][4];
  f32x2 x[4][4], cb1[4], sb2[4];
  {
    LAS const char* qp = ldsp(opaque(LB + L_Q + tg * 1088 + (8 * w + g8) * 16));
    v4u qv[4];
#pragma unroll
    for (int t = 0; t < 4; ++t) qv[t] = *(LAS const v4u*)(qp + t * 8704);
#define LDW(j) do { SCHED(); if (w < 4) { _Pragma("unroll") for (int i_ = 0; i_ < 4; ++i_) { \
      if ((j) < 4) wA[4 * (j) + i_] = ld128(wsW12, OFF_W1P + wo + (4 * (j) + i_) * 1024, 1); \
      else if (4 * ((j) - 4) + i_ < NWB) wB[4 * ((j) - 4) + i_] = ld128(wsW12, OFF_W2P + wo + (4 * ((j) - 4) + i_) * 1024, 1); } } SCHED(); } while (0)
    LDW(0);
#pragma unroll
    for (int t = 0; t < 4; ++t)
#pragma unroll
      for (int i = 0; i < 4; ++i) { x[t][i] = up(qv[t][i]); asm volatile("" : "+v"(x[t][i])); }
    LDW(1);
    BAR();
    const f32x4 sA = *(LAS const f32x4*)(L + L_S1 + tg * 32), sB = *(LAS const f32x4*)(L + L_S1 + tg * 32 + 16);
    const float mm[4] = {sA[0], sA[2], sB[0], sB[2]}, rr[4] = {sA[1], sA[3], sB[1], sB[3]};
    LAS const char* mp = ldsp(opaque(LB + L_MV + (64 * w + 8 * g8) * 4));
    f32x4 mv[5][2];
#pragma unroll
    for (int v = 0; v < 5; ++v) { mv[v][0] = *(LAS const f32x4*)(mp + v * 2048); mv[v][1] = *(LAS const f32x4*)(mp + v * 2048 + 16); }
#define MV2(v, i) f32x2{mv[v][(i) >> 1][2 * ((i) & 1)], mv[v][(i) >> 1][2 * ((i) & 1) + 1]}
#pragma unroll
    for (int i = 0; i < 4; ++i) {
      const f32x2 A = MV2(2, i) * MV2(1, i), B = MV2(2, i) * MV2(0, i);
#pragma unroll
      for (int t = 0; t < 4; ++t) {
        const f32x2 u = fma2(fma2(x[t][i], sp(rr[t]), sp(mm[t])), A, B);
        ghl[t][i] = pk(u.x, 0.f); ghh[t][i] = pk(0.f, u.y);
        asm volatile("" : "+v"(ghl[t][i]), "+v"(ghh[t][i]));
      }
      cb1[i] = MV2(4, i); sb2[i] = MV2(3, i);
      LDW(2 + i);
    }
    LDW(6); LDW(7);
  }
  LAS float* part = (LAS float*)ldsp(opaque(LB + L_PART + lane * 4));
  unsigned w3o;
  {
    const int n = min(w, 6) * 64 + lane, pos = (n * 4682) >> 16, pc = n - 14 * pos;
    w3o = opaque((unsigned)(((pos & 19) | ((pos & 4) << 1) | ((pos & 8) >> 1)) * 3584 + m * 224 + pc * 16));
  }
#define W3DMA(ks) do { SCHED(); if (!(BGOFF & 32) && w < 7) glds16s(w3o, (const char*)p.w3 + (ks) * (32 * 3584), LB + L_W3 + (ks) * 7168 + w * 1024); SCHED(); } while (0)
#define BG2(j) do { W3DMA(j); if (NWB + (j) < 16) { wB[(NWB + (j)) & 15] = ld128(wsW12, OFF_W2P + wo + (NWB + (j)) * 1024, 1); SCHED(); } } while (0)
  const unsigned zo = opaque(OFF_Z + T * 32768 + (32 * w + 4 * g8) * 128 + tg * 16);
  LAS char* xw = ldsp(opaque(LB + L_X + (tg >> 2) * 16384 + (8 * w + g8) * 256 + ((tg & 2) * 4 + (tg & 1)) * 16));
  STAMP(6);
  if (w == 7) poll4(ws, OFF_F1 + team * 8192 + lane * 128, tag);
  BAR();
  STAMP(7);
  f32x4 cs1 = {0.f, 0.f, 0.f, 0.f};
  {
    v4u zz[4];
#pragma unroll
    for (int i = 0; i < 4; ++i) zz[i] = ld128(ws, zo + i * 128, 1);
    SCHED();
#pragma unroll
    for (int i = 0; i < 16; ++i) cs1 = mfma(wA[i], ones, cs1);
    SCHED();
    f32x2 s2[4], q2[4];
#pragma unroll
    for (int i = 0; i < 4; ++i) {
#pragma unroll
      for (int t = 0; t < 4; ++t) {
        x[t][i].x = dot2(zz[i][t], ghl[t][i], x[t][i].x);
        x[t][i].y = dot2(zz[i][t], ghh[t][i], x[t][i].y);
      }
      if (i == 3) { BG2(0); }
#pragma unroll
      for (int t = 0; t < 4; ++t) {
        s2[t] = i ? s2[t] + x[t][i] : x[t][i];
        q2[t] = i ? fma2(x[t][i], x[t][i], q2[t]) : x[t][i] * x[t][i];
      }
      SCHED();
    }
    float s[4], sq[4];
#pragma unroll
    for (int t = 0; t < 4; ++t) { s[t] = s2[t].x + s2[t].y; sq[t] = q2[t].x + q2[t].y; }
    part[512 + w * 64] = ln_wave_reduce(s, sq, lane);
    BG2(1);
    BAR();
    STAMP(35);
    BG2(2);
    float mean, sd, mm[4], rr[4];
    ln_total(part + 512, lane, mean, sd);
    const float rstd = __builtin_amdgcn_rcpf(sd);
    gather4(-mean * rstd, mm); gather4(rstd, rr);
    BG2(3);
    const int sw = (tg >> 1) * 32;
#pragma unroll
    for (int t = 0; t < 4; ++t) {
      v4u o; s[t] = 0.f; sq[t] = 0.f;
#pragma unroll
      for (int i = 0; i < 4; ++i) {
        o[i] = pk2(fma2(fma2(x[t][i], sp(rr[t]), sp(mm[t])), cb1[i], sb2[i]));
        s[t] = dot2(o[i], 0x3F803F80u, s[t]); sq[t] = dot2(o[i], o[i], sq[t]);
      }
      *(LAS v4u*)(xw + ((t * 32) ^ sw)) = o;
      BG2(4 + t);
    }
    part[1024 + w * 64] = ln_wave_reduce(s, sq, lane);
    if ((w >> 1) == g) {
#pragma unroll
      for (int t = 0; t < 4; ++t)
        *(LAS v4u*)(L + L_X1 + (tg >> 2) * 4096 + ((w & 1) * 8 + g8) * 256 + ((tg & 2) * 4 + (tg & 1)) * 16 + ((t * 32) ^ sw)) =
            v4u{pk2(x[t][0]), pk2(x[t][1]), pk2(x[t][2]), pk2(x[t][3])};
    }
    BAR();
  }
  STAMP(8);
#define GEMM32(W, XA, XB, HOOK) do { \
    v4u xf[32]; \
    _Pragma("unroll") for (int j = 0; j < 12; ++j) xf[j] = *(LAS const v4u*)(((j & 1) ? XB : XA) + (j >> 1) * 1024); \
    SCHED(); \
    _Pragma("unroll") for (int j = 0; j < 32; ++j) { \
      if (j + 12 < 32) xf[j + 12] = *(LAS const v4u*)(((j & 1) ? XB : XA) + ((j + 12) >> 1) * 1024); \
      acc[j & 1] = mfma(W[j >> 1], xf[j], acc[j & 1]); \
      SCHED(); \
      HOOK; \
    } } while (0)
  v2u resh[2], resx[2];
  {
    const unsigned s0 = (c16 ^ (2 * (c16 >> 3))) * 16, s1 = (c16 ^ (4 + 2 * (c16 >> 3))) * 16;
    LAS const char* xa = ldsp(opaque(LB + L_X + qq * 256 + s0));
    LAS const char* xb = ldsp(opaque(LB + L_X + 16384 + qq * 256 + s1));
    f32x4 acc[2] = {{0.f, 0.f, 0.f, 0.f}, {0.f, 0.f, 0.f, 0.f}};
    float mean, sd, mm[2], sdv[2];
    GEMM32(wA, xa, xb, { if (j == 1 || j == 5 || j == 9 || j == 13) W3DMA(8 + (j >> 2));
                 if (j == 20) { ln_total(part + 1024, lane, mean, sd); SCHED(); }
                 if (j == 26) { const int src = 16 * ((c16 >> 1) & 3) + 2 * (c16 >> 3) + (c16 & 1);
                                mm[0] = __shfl(mean, src); sdv[0] = __shfl(sd, src); mm[1] = __shfl(mean, src + 4); sdv[1] = __shfl(sd, src + 4); SCHED(); } });
    STAMP(28);
    unsigned d[2][2];
#pragma unroll
    for (int tt = 0; tt < 2; ++tt) {
      const float r = __builtin_amdgcn_rcpf(sdv[tt]), k1 = -2.302208198f * r, k2 = -0.1029432397f * r * r * r;
      d[tt][0] = gelu_p(fma2(sp(-mm[tt]), f32x2{cs1[0], cs1[1]}, f32x2{acc[tt][0], acc[tt][1]}), k1, k2, sdv[tt]);
      d[tt][1] = gelu_p(fma2(sp(-mm[tt]), f32x2{cs1[2], cs1[3]}, f32x2{acc[tt][2], acc[tt][3]}), k1, k2, sdv[tt]);
    }
    swap16u(d[0][0], d[1][0]); swap16u(d[0][1], d[1][1]);
    st128s(v4u{d[0][0], d[0][1], d[1][0], d[1][1]}, ws, OFF_HH + (2 * T + (qq & 1)) * 16384 + (16 * g + 2 * w + (qq >> 1)) * 256 + c16 * 16);
    SCHED();
    STAMP(29);
#pragma unroll
    for (int tt = 0; tt < 2; ++tt) {
      resh[tt] = *(LAS const v2u*)(L + L_X + tt * 16384 + (16 * g + 2 * w + (qq >> 1)) * 256 + (tt ? s1 : s0) + (qq & 1) * 8);
      resx[tt] = *(LAS const v2u*)(L + L_X1 + tt * 4096 + (2 * w + (qq >> 1)) * 256 + (tt ? s1 : s0) + (qq & 1) * 8);
    }
  }
  unsigned ho[2];
  {
    const int gk = w >> 1, shift = gk == 0 ? -1 : gk == 1 ? 1 : gk == 2 ? -2 : 2;
#pragma unroll
    for (int tt = 0; tt < 2; ++tt) {
      const int tok = 32 * T + 16 * tt + c16 + shift;
      ho[tt] = opaque((tok >= 0 && tok < 2048) ? OFF_HH + (tok >> 4) * 16384 + 2 * w * 1024 + qq * 256 + (tok & 15) * 16 : OOB);
    }
  }
  DRAIN();
  BAR();
  if (tid == 0) st32s(tag, ws, OFF_F2 + me2 * 128);
  STAMP(9);
  const int th = m >> 3, cj = m & 7;
  v4u w4[4][7];
  const unsigned w4o = OFF_W4P + (4 * cj * 56 + 7 * w) * 1024 + lane * 16;
  const f32x4 gbv = *(LAS const f32x4*)(L + L_GB + (128 * g + 16 * w + 4 * qq) * 4);
  W3DMA(12); W3DMA(13); W3DMA(14); W3DMA(15);
#pragma unroll
  for (int i = 0; i < 3; ++i) w4[2][i] = ld128(wsW4, w4o + (112 + i) * 1024, 1);
  SCHED();
  LAS char* xr = ldsp(opaque(LB + L_X + lane * 16));
  if (w == 7) {
    const int j = lane & 7, dT = j < 4 ? 0 : ((j & 1) ? 1 : -1), Tn = min(max(T + dT, 0), 63);
    poll1(ws, OFF_F2 + (Tn * 4 + (j & 3)) * 128, tag);
  }
  BAR();
  STAMP(10);
  {
    v4u xv[2][2];
#pragma unroll
    for (int tt = 0; tt < 2; ++tt) { xv[tt][0] = ld128(ws, ho[tt], 1); xv[tt][1] = ld128(ws, ho[tt] + 1024, 1); }
    SCHED();
#pragma unroll
    for (int tt = 0; tt < 2; ++tt)
#pragma unroll
      for (int i = 0; i < 2; ++i) *(LAS v4u*)(xr + tt * 16384 + (2 * w + i) * 1024) = xv[tt][i];
    BAR();
    STAMP(30);
    f32x4 acc[2] = {{0.f, 0.f, 0.f, 0.f}, {0.f, 0.f, 0.f, 0.f}};
    GEMM32(wB, xr, (xr + 16384), { if ((j & 3) == 1 && j < 28) { w4[0][j >> 2] = ld128(wsW4, w4o + (j >> 2) * 1024, 1); SCHED(); } });
    STAMP(31);
    unsigned d[2][2];
    f32x4 r[2];
    float ss[2], sq[2];
#pragma unroll
    for (int tt = 0; tt < 2; ++tt) {
      d[tt][0] = pk2(up(resh[tt].x) + f32x2{acc[tt][0], acc[tt][1]});
      d[tt][1] = pk2(up(resh[tt].y) + f32x2{acc[tt][2], acc[tt][3]});
      ss[tt] = dot2(d[tt][1], 0x3F803F80u, dot2(d[tt][0], 0x3F803F80u, 0.f));
      sq[tt] = dot2(d[tt][1], d[tt][1], dot2(d[tt][0], d[tt][0], 0.f));
    }
    unsigned e[2][2] = {{d[0][0], d[0][1]}, {d[1][0], d[1][1]}};
    swap16u(e[0][0], e[1][0]); swap16u(e[0][1], e[1][1]);
    st128s(v4u{e[0][0], e[0][1], e[1][0], e[1][1]}, ws, OFF_X1P + (2 * T + (qq & 1)) * 16384 + (16 * g + 2 * w + (qq >> 1)) * 256 + c16 * 16);
    swap32(ss[0], ss[1]); swap32(sq[0], sq[1]);
    float sa = ss[0] + ss[1], sb = sq[0] + sq[1];
    swap16(sa, sb);
    st32s(ubits(sa + sb), ws, OFF_ST + ((2 * T + (lane >> 5)) * 32 + 8 * g + w) * 128 + (lane & 31) * 4);
    SCHED();
#pragma unroll
    for (int tt = 0; tt < 2; ++tt) {
      const f32x2 r0 = fma2(f32x2{gbv[0], gbv[1]}, up(d[tt][0]), up(resx[tt].x)), r1 = fma2(f32x2{gbv[2], gbv[3]}, up(d[tt][1]), up(resx[tt].y));
      r[tt] = f32x4{r0.x, r0.y, r1.x, r1.y};
    }
    DRAIN();
    BAR();
    if (tid == 0) st32s(tag, ws, OFF_F3 + me2 * 128);
    STAMP(11);
#pragma unroll
    for (int tt = 0; tt < 2; ++tt) st128s(__builtin_bit_cast(v4u, r[tt]), ws, OFF_R + ((2 * T + tt) * 32 + 8 * g + w) * 1024 + lane * 16);
  }
#pragma unroll
  for (int i = 0; i < 7; ++i) w4[1][i] = ld128(wsW4, w4o + (56 + i) * 1024, 1);
  SCHED();
  {
    LAS char* a = ldsp(opaque(LB + L_W3 + w * 14336 + (16 * (qq >> 1) + 4 * (qq & 1) + (c16 >> 2)) * 224 + (lane & 3) * 8));
    LAS char* o = ldsp(opaque(LB + L_W3 + w * 14336 + lane * 16));
    v4u f[14];
#pragma unroll
    for (int j = 0; j < 14; ++j) {
      const v2u t0 = trread(a + (j / 7) * 7168 + (j % 7) * 32), t1 = trread(a + (j / 7) * 7168 + (j % 7) * 32 + 1792);
      f[j] = v4u{t0.x, t0.y, t1.x, t1.y};
    }
    asm volatile("s_waitcnt lgkmcnt(0)" ::: "memory");
    SCHED();
    f32x4 cs = {0.f, 0.f, 0.f, 0.f};
#pragma unroll
    for (int j = 0; j < 14; ++j) {
      *(LAS v4u*)(o + j * 1024) = f[j];
      const unsigned e = c16 == j % 7 ? 0x3F803F80u : 0u;
      cs = mfma(f[j], v4u{e, e, e, e}, cs);
    }
    *(LAS f32x4*)(L + L_X + w * 1024 + lane * 16) = cs;
  }
  if (w == 7) poll1(ws, OFF_F3 + (16 * squad + c16) * 128, tag);
  const unsigned xo4 = opaque(OFF_X1P + (8 * squad + w) * 16384 + lane * 16);
  LAS const char* w3a = ldsp(opaque(LB + L_W3 + lane * 16));
  LAS const char* w3b = ldsp(opaque(LB + L_W3 + lane * 16 + 8 * 7168));
#define W3FRAG(i, n) (*(LAS const v4u*)(((i) < 8 ? w3a : w3b) + ((i) & 7) * 7168 + (n) * 1024))
  BAR();
  STAMP(12);
  {
    v4u xv[16], sv[4], wf[2][7];
#pragma unroll
    for (int i = 0; i < 8; ++i) xv[i] = ld128(ws, xo4 + i * 1024, 1);
    SCHED();
    STAMP(32);
    f32x4 cs = {0.f, 0.f, 0.f, 0.f};
#pragma unroll
    for (int v = 0; v < 8; ++v) cs += *(LAS const f32x4*)(xr + v * 1024);
    LAS char* csw = ldsp(opaque(LB + L_X + 8192 + w * 1024 + c16 * 64 + qq * 16));
    *(LAS f32x4*)(csw) = cs;
#pragma unroll
    for (int n = 0; n < 7; ++n) wf[0][n] = W3FRAG(0, n);
    SCHED();
    f32x4 acc[7];
#pragma unroll
    for (int n = 0; n < 7; ++n) acc[n] = f32x4{0.f, 0.f, 0.f, 0.f};
#pragma unroll
    for (int i = 0; i < 16; ++i) {
      if (i < 8) xv[i + 8] = ld128(ws, xo4 + (i + 8) * 1024, 1);
      else if (i == 8) {
#pragma unroll
        for (int j = 0; j < 4; ++j) sv[j] = ld128(ws, OFF_ST + (8 * squad + w) * 4096 + j * 1024 + lane * 16, 1);
      } else if (i < 13) {
        w4[2][i - 6] = ld128(wsW4, w4o + (112 + i - 6) * 1024, 1);
      } else {
        w4[3][i - 13] = ld128(wsW4, w4o + (168 + i - 13) * 1024, 1);
      }
      SCHED();
#pragma unroll
      for (int n = 0; n < 7; ++n) {
        if (i < 15) wf[(i + 1) & 1][n] = W3FRAG(i + 1, n);
        acc[n] = mfma(wf[i & 1][n], xv[i], acc[n]);
        SCHED();
      }
    }
    STAMP(33);
    STAMP(21);
    LAS const char* csp = ldsp(opaque(LB + L_X + 8192 + w * 1024 + qq * 16));
    f32x4 c3[7];
#pragma unroll
    for (int n = 0; n < 7; ++n) c3[n] = *(LAS const f32x4*)(csp + n * 64);
    float mean, sd;
    {
      float t4[4];
#pragma unroll
      for (int j = 0; j < 4; ++j) { t4[j] = (fbits(sv[0][j]) + fbits(sv[1][j])) + (fbits(sv[2][j]) + fbits(sv[3][j])); asm("" : "+v"(t4[j])); }
      float a = t4[0], bb = t4[2], c = t4[1], dd2 = t4[3];
      swap32(a, bb); swap32(c, dd2);
      float e = a + bb, f = c + dd2;
      swap16(e, f);
      float h = e + f;
      h += ror8(h);
      const int src = ((c16 >> 1) & 1) * 32 + (c16 & 1) * 16 + (c16 >> 2);
      float s1 = __shfl(h, src), s2 = __shfl(h, src + 4);
      asm("" : "+v"(s1)); asm("" : "+v"(s2));
      ln_finish(s1, s2, mean, sd);
    }
    const float rs = __builtin_amdgcn_rcpf(sd), k1 = -2.302208198f * rs, k2 = -0.1029432397f * rs * rs * rs;
    const unsigned go = OFF_G + (8 * squad + w) * 57344 + m * 3584;
    unsigned d[7][2];
#pragma unroll
    for (int n = 0; n < 7; ++n) {
      if (n < 4) w4[3][3 + n] = ld128(wsW4, w4o + (168 + 3 + n) * 1024, 1);
      SCHED();
      d[n][0] = gelu_p(fma2(sp(-mean), f32x2{c3[n][0], c3[n][1]}, f32x2{acc[n][0], acc[n][1]}), k1, k2, sd);
      d[n][1] = gelu_p(fma2(sp(-mean), f32x2{c3[n][2], c3[n][3]}, f32x2{acc[n][2], acc[n][3]}), k1, k2, sd);
      if (n == 1 || n == 3 || n == 5) st128s(v4u{d[n - 1][0], d[n - 1][1], d[n][0], d[n][1]}, ws, go + (n >> 1) * 1024 + lane * 16);
      if (n == 6) {
        unsigned x0 = d[6][0], y0 = d[6][0], x1 = d[6][1], y1 = d[6][1];
        swap16u(x0, y0); swap16u(x1, y1);
        st128s(v4u{x0, x1, y0, y1}, ws, (qq & 1) ? OOB : go + 3072 + (qq >> 1) * 256 + c16 * 16);
      }
    }
    STAMP(13);
  }
  DRAIN();
  BAR();
  if (tid == 0) st32s(tag, ws, OFF_F4 + me2 * 128);
  STAMP(14);
  const unsigned xo = opaque(OFF_G + (8 * squad + 4 * th) * 57344 + 7 * w * 1024 + lane * 16);
  LAS char* rw0 = ldsp(opaque(LB + w * 16384 + qq * 256 + ((c16 + 4 * (qq >> 1)) & 15) * 16));
  LAS char* rw1 = ldsp(opaque(LB + w * 16384 + qq * 256 + ((c16 + 8 + 4 * (qq >> 1)) & 15) * 16));
  if (w == 7) poll1(ws, OFF_F4 + (16 * squad + c16) * 128, tag);
  BAR();
  STAMP(15);
  {
    v4u xv[2][7];
#pragma unroll
    for (int i = 0; i < 7; ++i) xv[0][i] = ld128(ws, xo + i * 1024, 1);
    const unsigned ro = OFF_R + ((8 * squad + 4 * th + (w >> 1)) * 32 + 4 * cj + 2 * (w & 1)) * 1024 + lane * 16;
    v4u r0, r1;
    SCHED();
#pragma unroll
    for (int tt = 0; tt < 4; ++tt) {
      f32x4 acc[4];
#pragma unroll
      for (int n = 0; n < 4; ++n) acc[n] = f32x4{0.f, 0.f, 0.f, 0.f};
#pragma unroll
      for (int i = 0; i < 7; ++i) {
        if (tt < 3) xv[(tt + 1) & 1][i] = ld128(ws, xo + (tt + 1) * 57344 + i * 1024, 1);
        if (tt == 3 && i == 0) { r0 = ld128(ws, ro, 1); r1 = ld128(ws, ro + 1024, 1); }
        SCHED();
#pragma unroll
        for (int n = 0; n < 4; ++n) acc[n] = mfma(w4[n][i], xv[tt & 1][i], acc[n]);
        SCHED();
      }
#pragma unroll
      for (int n = 0; n < 4; ++n) *(LAS f32x4*)(((n & 1) ? rw1 : rw0) + (tt * 4 + n) * 1024) = acc[n];
      SCHED();
    }
    *(LAS v4u*)(rw0 + (131072 - w * 14336)) = r0;
    *(LAS v4u*)(rw1 + (131072 + 1024 - w * 14336)) = r1;
    const unsigned ra = LB + ((w >> 1) * 4 + (tg >> 1)) * 1024 + (tg & 1) * 512 + ((8 * (w & 1) + g8 + 4 * tg) & 15) * 16;
    LAS const char* rr = ldsp(opaque(ra));
    LAS const char* rr2 = ldsp(opaque(ra + 65536));
    LAS const char* rr3 = ldsp(opaque(ra + 131072));
    BAR();
    STAMP(16);
    f32x4 s0 = {0.f, 0.f, 0.f, 0.f}, s1 = {0.f, 0.f, 0.f, 0.f};
#pragma unroll
    for (int v = 0; v < 8; ++v) {
      s0 += *(LAS const f32x4*)((v < 4 ? rr : rr2) + (v & 3) * 16384);
      s1 += *(LAS const f32x4*)((v < 4 ? rr : rr2) + (v & 3) * 16384 + 256);
    }
    const f32x4 q0 = *(LAS const f32x4*)(rr3), q1 = *(LAS const f32x4*)(rr3 + 256);
    const f32x4 o0 = *(LAS const f32x4*)(L + L_GB + (64 * cj + 8 * tg) * 4) * s0 + q0, o1 = *(LAS const f32x4*)(L + L_GB + (64 * cj + 8 * tg) * 4 + 16) * s1 + q1;
    const v4u o = {pk(o0[0], o0[1]), pk(o0[2], o0[3]), pk(o1[0], o1[1]), pk(o1[2], o1[3])};
    st128s(o, mkrs(p.out, 2048 * 1024), (128 * squad + 64 * th + 8 * w + g8) * 1024 + 128 * cj + 16 * tg);
  }
  if (b == 0 && tid == 0) st32s(tag, ws, OFF_EPOCH);
  STAMP(17);
}
}

extern "C" void kernel_launch(void* const* d_in, const int* in_sizes, int n_in, void* d_out, int out_size, void* d_ws,
                              size_t ws_size, hipStream_t stream) {
  mk::Params p;
  p.q = (const mk::u16*)d_in[0]; p.k = (const mk::u16*)d_in[1]; p.v = (const mk::u16*)d_in[2];
  p.sk = (const mk::u16*)d_in[3]; p.ssmk = (const mk::u16*)d_in[4]; p.ssmD = (const mk::u16*)d_in[5];
  p.w1 = (const mk::u16*)d_in[7]; p.w2 = (const mk::u16*)d_in[8]; p.w3 = (const mk::u16*)d_in[9]; p.w4 = (const mk::u16*)d_in[10];
  p.c = (const mk::u16*)d_in[11]; p.wmod = (const mk::u16*)d_in[12];
  p.out = (mk::u16*)d_out; p.ws = (char*)d_ws;
  mk::mega<<<256, 512, 0, stream>>>(p);
}
```
